```python
import torch

def _softmax(x, axis=-1):
    return torch.softmax(x, dim=axis)
def _gelu(x):
    return torch.nn.functional.gelu(x, approximate="tanh")
def _silu(x):
    return torch.nn.functional.silu(x)
def _sigmoid(x):
    return torch.sigmoid(x)
def _layernorm(x, axis=-1, eps=1e-5):
    m = x.mean(dim=axis, keepdim=True)
    v = x.var(dim=axis, keepdim=True, unbiased=False)
    return (x - m) / torch.sqrt(v + eps)
def _scatter_add(tgt, idx, src, axis=0):
    return tgt.clone().index_add_(axis, idx, src)
def _segment_sum(src, seg, n):
    out = torch.zeros((n,) + src.shape[1:], dtype=src.dtype)
    return out.index_add_(0, seg, src)
def _pad(x, pw):
    flat = tuple(v for pair in reversed(pw) for v in pair)
    return torch.nn.functional.pad(x, flat)

kernel_name = 'gated_mix__mha_block_cached__glu_gated_bilinear__decode'

def setup_inputs():

    _w = (lambda a: (lambda t: t.to(torch.bfloat16) if t.is_floating_point() else t)(torch.as_tensor(a)))
    def _A_setup():
        B, Sq, D = 4, 1, 640
        H, Sk, Dh = 32, 4096, 128
        return {
            "x": _w((lambda *s: torch.randn(*s, dtype=torch.bfloat16) * 0.05)(B, Sq, D)),
            "k_cache": _w((lambda *s: torch.randn(*s, dtype=torch.bfloat16) * 6.4)(B, H, Sk, Dh)),
            "v_cache": _w((lambda *s: torch.randn(*s, dtype=torch.bfloat16) * 0.05)(B, H, Sk, Dh)),
            "wq": _w((lambda *s: torch.randn(*s, dtype=torch.bfloat16) * 6.4)(D, H * Dh) * (D ** -0.5)),
            "wo": _w((lambda *s: torch.randn(*s, dtype=torch.bfloat16) * 0.05)(H * Dh, D) * ((H * Dh) ** -0.5)),
        }
    _A_in = _A_setup()

    def _B_setup():
        B, S, D, F = 4, 1, 640, 1728
        return {
            "x": _w((lambda *s: torch.randn(*s, dtype=torch.bfloat16) * 0.05)(B, S, D)),
            "w_gate": _w((lambda *s: torch.randn(*s, dtype=torch.bfloat16) * 0.4)(D, F) * (D ** -0.5)),
            "w_up": _w((lambda *s: torch.randn(*s, dtype=torch.bfloat16) * 0.4)(D, F) * (D ** -0.5)),
            "w_down": _w((lambda *s: torch.randn(*s, dtype=torch.bfloat16) * 0.4)(F, D) * (F ** -0.5)),
        }
    _B_in = _B_setup()

    return {
        "A_x": _A_in["x"],
        "A_k_cache": _A_in["k_cache"],
        "A_v_cache": _A_in["v_cache"],
        "A_wq": _A_in["wq"],
        "A_wo": _A_in["wo"],
        "B_x": _B_in["x"],
        "B_w_gate": _B_in["w_gate"],
        "B_w_up": _B_in["w_up"],
        "B_w_down": _B_in["w_down"],
        "gate_w": _w((lambda *s: torch.randn(*s, dtype=torch.bfloat16) * 0.05)(640) * 16.0),
    }

def reference(A_x, A_k_cache, A_v_cache, A_wq, A_wo, B_x, B_w_gate, B_w_up, B_w_down, gate_w):
    def _A_ref(x, k_cache, v_cache, wq, wo):
        B, Sq, D = x.shape
        H, Dh = 32, 128
        q = (lambda x, *d: x.permute(*d))((x @ wq).reshape(B, Sq, H, Dh), 0, 2, 1, 3)
        scores = torch.einsum("bhqd,bhkd->bhqk", q, k_cache) * (Dh ** -0.5)
        p = _softmax(scores, axis=-1)
        o = torch.einsum("bhqk,bhkd->bhqd", p, v_cache)
        return (lambda x, *d: x.permute(*d))(o, 0, 2, 1, 3).reshape(B, Sq, H * Dh) @ wo

    def _B_ref(x, w_gate, w_up, w_down):
        g = torch.einsum("bsd,df->bsf", x, w_gate)
        u = torch.einsum("bsd,df->bsf", x, w_up)
        h = g * u
        return torch.einsum("bsf,fd->bsd", h, w_down)

    x = A_x
    g = _sigmoid((x * gate_w).sum(-1, keepdims=True))
    a = _A_ref(x, A_k_cache, A_v_cache, A_wq, A_wo)
    b = _B_ref(x, B_w_gate, B_w_up, B_w_down)
    return g * a + (1 - g) * b
```

```cpp
#include <hip/hip_runtime.h>
#include <hip/hip_bf16.h>
#include <cstdint>

#define MIMIC 0
constexpr int B = 4, D = 640, H = 32, SK = 4096, DH = 128, F = 1728;
constexpr int NS = 8;
using u32x4 = __attribute__((ext_vector_type(4))) unsigned;

__device__ __forceinline__ float bf2f(unsigned short h) { return __uint_as_float((unsigned)h << 16); }
__device__ __forceinline__ float rbf(float x) { return __bfloat162float(__float2bfloat16(x)); }

__global__ void k1(const unsigned short* x, const unsigned short* wq, const unsigned short* wg, const unsigned short* wu,
                   float* q, float* h) {
  __shared__ float xs[B * D];
  for (int i = threadIdx.x; i < B * D; i += blockDim.x) xs[i] = bf2f(x[i]);
  __syncthreads();
  int n = blockIdx.x * blockDim.x + threadIdx.x;
  if (n < H * DH) {
    float a[B] = {0, 0, 0, 0};
    for (int k = 0; k < D; ++k) { float w = bf2f(wq[(size_t)k * H * DH + n]);
      for (int b = 0; b < B; ++b) a[b] = fmaf(xs[b * D + k], w, a[b]); }
    for (int b = 0; b < B; ++b) q[b * H * DH + n] = rbf(a[b]);
  } else if (n < H * DH + F) {
    int f = n - H * DH;
    float g[B] = {0, 0, 0, 0}, u[B] = {0, 0, 0, 0};
    for (int k = 0; k < D; ++k) { float w1 = bf2f(wg[(size_t)k * F + f]), w2 = bf2f(wu[(size_t)k * F + f]);
      for (int b = 0; b < B; ++b) { g[b] = fmaf(xs[b * D + k], w1, g[b]); u[b] = fmaf(xs[b * D + k], w2, u[b]); } }
    for (int b = 0; b < B; ++b) h[b * F + f] = MIMIC ? rbf(rbf(g[b]) * rbf(u[b])) : g[b] * u[b];
  }
}

__global__ void k2(const float* q, const unsigned short* kc, const unsigned short* vc, float* pm, float* pl, float* po) {
  constexpr int KEYS = SK / NS;
  __shared__ float s[KEYS];
  __shared__ float qs[DH];
  __shared__ float red[256];
  __shared__ float ored[2][DH];
  int bh = blockIdx.x / NS, sp = blockIdx.x % NS, t = threadIdx.x;
  if (t < DH) qs[t] = q[bh * DH + t];
  __syncthreads();
  const unsigned short* kb = kc + ((size_t)bh * SK + sp * KEYS) * DH;
  const unsigned short* vb = vc + ((size_t)bh * SK + sp * KEYS) * DH;
  float mx = -INFINITY;
  for (int j = t; j < KEYS; j += 256) {
    float a = 0;
    for (int d = 0; d < DH; ++d) a = fmaf(qs[d], bf2f(kb[(size_t)j * DH + d]), a);
    a = MIMIC ? rbf(rbf(a) * 0.08838834764831845f) : a * 0.08838834764831845f;
    s[j] = a; mx = fmaxf(mx, a);
  }
  red[t] = mx; __syncthreads();
  for (int o = 128; o > 0; o >>= 1) { if (t < o) red[t] = fmaxf(red[t], red[t + o]); __syncthreads(); }
  mx = red[0]; __syncthreads();
  float l = 0;
  for (int j = t; j < KEYS; j += 256) { float p = __expf(s[j] - mx); s[j] = p; l += p; }
  red[t] = l; __syncthreads();
  for (int o = 128; o > 0; o >>= 1) { if (t < o) red[t] += red[t + o]; __syncthreads(); }
  l = red[0];
  int d = t & 127, half = t >> 7;
  float o = 0;
  for (int j = half; j < KEYS; j += 2) o = fmaf(s[j], bf2f(vb[(size_t)j * DH + d]), o);
  ored[half][d] = o; __syncthreads();
  if (t < DH) po[(size_t)blockIdx.x * DH + t] = ored[0][t] + ored[1][t];
  if (t == 0) { pm[blockIdx.x] = mx; pl[blockIdx.x] = l; }
}

__global__ void k3(const unsigned short* x, const unsigned short* gw, const float* pm, const float* pl, const float* po, const float* h,
                   const unsigned short* wo, const unsigned short* wd, __hip_bfloat16* out) {
  __shared__ float os[H * DH];
  __shared__ float hs[F];
  __shared__ float ra[4][64], rb[4][64];
  __shared__ float red[256];
  int b = blockIdx.x, c0 = blockIdx.y * 64, t = threadIdx.x;
  for (int i = t; i < H * DH; i += 256) {
    int hh = i / DH, d = i % DH; int base = (b * H + hh) * NS;
    float M = -INFINITY; for (int s = 0; s < NS; ++s) M = fmaxf(M, pm[base + s]);
    float L = 0, o = 0;
    for (int s = 0; s < NS; ++s) { float w = __expf(pm[base + s] - M); L += w * pl[base + s]; o += w * po[(size_t)(base + s) * DH + d]; }
    os[i] = MIMIC ? rbf(o / L) : o / L;
  }
  for (int i = t; i < F; i += 256) hs[i] = h[b * F + i];
  float gp = 0;
  for (int i = t; i < D; i += 256) { float v = bf2f(x[b * D + i]) * bf2f(gw[i]); gp += MIMIC ? rbf(v) : v; }
  red[t] = gp; __syncthreads();
  for (int o = 128; o > 0; o >>= 1) { if (t < o) red[t] += red[t + o]; __syncthreads(); }
  float gs = red[0];
  int c = t & 63, kg = t >> 6;
  float a = 0, bb = 0;
  for (int k = kg; k < H * DH; k += 4) a = fmaf(os[k], bf2f(wo[(size_t)k * D + c0 + c]), a);
  for (int k = kg; k < F; k += 4) bb = fmaf(hs[k], bf2f(wd[(size_t)k * D + c0 + c]), bb);
  ra[kg][c] = a; rb[kg][c] = bb; __syncthreads();
  if (t < 64) {
    a = ra[0][c] + ra[1][c] + ra[2][c] + ra[3][c]; bb = rb[0][c] + rb[1][c] + rb[2][c] + rb[3][c];
    float r;
    if (MIMIC) { gs = rbf(gs); float g = rbf(1.f / (1.f + __expf(-gs))); a = rbf(a); bb = rbf(bb);
      r = rbf(g * a) + rbf(rbf(1.f - g) * bb); }
    else { float g = 1.f / (1.f + __expf(-gs)); r = g * a + (1.f - g) * bb; }
    out[b * D + c0 + c] = __float2bfloat16(r);
  }
}

extern "C" void kernel_launch(void* const* d_in, const int* in_sizes, int n_in, void* d_out, int out_size, void* d_ws, size_t ws_size,
                              hipStream_t stream) {
  const unsigned short* x = (const unsigned short*)d_in[0];
  const unsigned short* kc = (const unsigned short*)d_in[1];
  const unsigned short* vc = (const unsigned short*)d_in[2];
  const unsigned short* wq = (const unsigned short*)d_in[3];
  const unsigned short* wo = (const unsigned short*)d_in[4];
  const unsigned short* wg = (const unsigned short*)d_in[6];
  const unsigned short* wu = (const unsigned short*)d_in[7];
  const unsigned short* wd = (const unsigned short*)d_in[8];
  const unsigned short* gw = (const unsigned short*)d_in[9];
  float* ws = (float*)d_ws;
  float* q = ws; float* h = q + B * H * DH; float* pm = h + B * F; float* pl = pm + B * H * NS; float* po = pl + B * H * NS;
  k1<<<(H * DH + F + 63) / 64, 64, 0, stream>>>(x, wq, wg, wu, q, h);
  k2<<<B * H * NS, 256, 0, stream>>>(q, kc, vc, pm, pl, po);
  k3<<<dim3(B, D / 64), 256, 0, stream>>>(x, gw, pm, pl, po, h, wo, wd, (__hip_bfloat16*)d_out);
}
```

```cpp
#define M_S1 1
#define M_S2 1
#define M_O 0
#define M_FFN 0
#define M_FIN 0
#include <hip/hip_runtime.h>
#include <hip/hip_bf16.h>
#include <cstdint>

#define MIMIC 1
constexpr int B = 4, D = 640, H = 32, SK = 4096, DH = 128, F = 1728;
constexpr int NS = 8;
using u32x4 = __attribute__((ext_vector_type(4))) unsigned;

__device__ __forceinline__ float bf2f(unsigned short h) { return __uint_as_float((unsigned)h << 16); }
__device__ __forceinline__ float rbf(float x) { return __bfloat162float(__float2bfloat16(x)); }

__global__ void k1(const unsigned short* x, const unsigned short* wq, const unsigned short* wg, const unsigned short* wu,
                   float* q, float* h) {
  __shared__ float xs[B * D];
  for (int i = threadIdx.x; i < B * D; i += blockDim.x) xs[i] = bf2f(x[i]);
  __syncthreads();
  int n = blockIdx.x * blockDim.x + threadIdx.x;
  if (n < H * DH) {
    float a[B] = {0, 0, 0, 0};
    for (int k = 0; k < D; ++k) { float w = bf2f(wq[(size_t)k * H * DH + n]);
      for (int b = 0; b < B; ++b) a[b] = fmaf(xs[b * D + k], w, a[b]); }
    for (int b = 0; b < B; ++b) q[b * H * DH + n] = rbf(a[b]);
  } else if (n < H * DH + F) {
    int f = n - H * DH;
    float g[B] = {0, 0, 0, 0}, u[B] = {0, 0, 0, 0};
    for (int k = 0; k < D; ++k) { float w1 = bf2f(wg[(size_t)k * F + f]), w2 = bf2f(wu[(size_t)k * F + f]);
      for (int b = 0; b < B; ++b) { g[b] = fmaf(xs[b * D + k], w1, g[b]); u[b] = fmaf(xs[b * D + k], w2, u[b]); } }
    for (int b = 0; b < B; ++b) h[b * F + f] = M_FFN ? rbf(rbf(g[b]) * rbf(u[b])) : g[b] * u[b];
  }
}

__global__ void k2(const float* q, const unsigned short* kc, const unsigned short* vc, float* pm, float* pl, float* po) {
  constexpr int KEYS = SK / NS;
  __shared__ float s[KEYS];
  __shared__ float qs[DH];
  __shared__ float red[256];
  __shared__ float ored[2][DH];
  int bh = blockIdx.x / NS, sp = blockIdx.x % NS, t = threadIdx.x;
  if (t < DH) qs[t] = q[bh * DH + t];
  __syncthreads();
  const unsigned short* kb = kc + ((size_t)bh * SK + sp * KEYS) * DH;
  const unsigned short* vb = vc + ((size_t)bh * SK + sp * KEYS) * DH;
  float mx = -INFINITY;
  for (int j = t; j < KEYS; j += 256) {
    float a = 0;
    for (int d = 0; d < DH; ++d) a = fmaf(qs[d], bf2f(kb[(size_t)j * DH + d]), a);
    if (M_S1) a = rbf(a); a = a * 0.08838834764831845f; if (M_S2) a = rbf(a);
    s[j] = a; mx = fmaxf(mx, a);
  }
  red[t] = mx; __syncthreads();
  for (int o = 128; o > 0; o >>= 1) { if (t < o) red[t] = fmaxf(red[t], red[t + o]); __syncthreads(); }
  mx = red[0]; __syncthreads();
  float l = 0;
  for (int j = t; j < KEYS; j += 256) { float p = __expf(s[j] - mx); s[j] = p; l += p; }
  red[t] = l; __syncthreads();
  for (int o = 128; o > 0; o >>= 1) { if (t < o) red[t] += red[t + o]; __syncthreads(); }
  l = red[0];
  int d = t & 127, half = t >> 7;
  float o = 0;
  for (int j = half; j < KEYS; j += 2) o = fmaf(s[j], bf2f(vb[(size_t)j * DH + d]), o);
  ored[half][d] = o; __syncthreads();
  if (t < DH) po[(size_t)blockIdx.x * DH + t] = ored[0][t] + ored[1][t];
  if (t == 0) { pm[blockIdx.x] = mx; pl[blockIdx.x] = l; }
}

__global__ void k3(const unsigned short* x, const unsigned short* gw, const float* pm, const float* pl, const float* po, const float* h,
                   const unsigned short* wo, const unsigned short* wd, __hip_bfloat16* out) {
  __shared__ float os[H * DH];
  __shared__ float hs[F];
  __shared__ float ra[4][64], rb[4][64];
  __shared__ float red[256];
  int b = blockIdx.x, c0 = blockIdx.y * 64, t = threadIdx.x;
  for (int i = t; i < H * DH; i += 256) {
    int hh = i / DH, d = i % DH; int base = (b * H + hh) * NS;
    float M = -INFINITY; for (int s = 0; s < NS; ++s) M = fmaxf(M, pm[base + s]);
    float L = 0, o = 0;
    for (int s = 0; s < NS; ++s) { float w = __expf(pm[base + s] - M); L += w * pl[base + s]; o += w * po[(size_t)(base + s) * DH + d]; }
    os[i] = M_O ? rbf(o / L) : o / L;
  }
  for (int i = t; i < F; i += 256) hs[i] = h[b * F + i];
  float gp = 0;
  for (int i = t; i < D; i += 256) { float v = bf2f(x[b * D + i]) * bf2f(gw[i]); gp += M_FIN ? rbf(v) : v; }
  red[t] = gp; __syncthreads();
  for (int o = 128; o > 0; o >>= 1) { if (t < o) red[t] += red[t + o]; __syncthreads(); }
  float gs = red[0];
  int c = t & 63, kg = t >> 6;
  float a = 0, bb = 0;
  for (int k = kg; k < H * DH; k += 4) a = fmaf(os[k], bf2f(wo[(size_t)k * D + c0 + c]), a);
  for (int k = kg; k < F; k += 4) bb = fmaf(hs[k], bf2f(wd[(size_t)k * D + c0 + c]), bb);
  ra[kg][c] = a; rb[kg][c] = bb; __syncthreads();
  if (t < 64) {
    a = ra[0][c] + ra[1][c] + ra[2][c] + ra[3][c]; bb = rb[0][c] + rb[1][c] + rb[2][c] + rb[3][c];
    float r;
    if (M_FIN) { gs = rbf(gs); float g = rbf(1.f / (1.f + __expf(-gs))); a = rbf(a); bb = rbf(bb);
      r = rbf(g * a) + rbf(rbf(1.f - g) * bb); }
    else { float g = 1.f / (1.f + __expf(-gs)); r = g * a + (1.f - g) * bb; }
    out[b * D + c0 + c] = __float2bfloat16(r);
  }
}

extern "C" void kernel_launch(void* const* d_in, const int* in_sizes, int n_in, void* d_out, int out_size, void* d_ws, size_t ws_size,
                              hipStream_t stream) {
  const unsigned short* x = (const unsigned short*)d_in[0];
  const unsigned short* kc = (const unsigned short*)d_in[1];
  const unsigned short* vc = (const unsigned short*)d_in[2];
  const unsigned short* wq = (const unsigned short*)d_in[3];
  const unsigned short* wo = (const unsigned short*)d_in[4];
  const unsigned short* wg = (const unsigned short*)d_in[6];
  const unsigned short* wu = (const unsigned short*)d_in[7];
  const unsigned short* wd = (const unsigned short*)d_in[8];
  const unsigned short* gw = (const unsigned short*)d_in[9];
  float* ws = (float*)d_ws;
  float* q = ws; float* h = q + B * H * DH; float* pm = h + B * F; float* pl = pm + B * H * NS; float* po = pl + B * H * NS;
  k1<<<(H * DH + F + 63) / 64, 64, 0, stream>>>(x, wq, wg, wu, q, h);
  k2<<<B * H * NS, 256, 0, stream>>>(q, kc, vc, pm, pl, po);
  k3<<<dim3(B, D / 64), 256, 0, stream>>>(x, gw, pm, pl, po, h, wo, wd, (__hip_bfloat16*)d_out);
}
```

```cpp
#include <hip/hip_runtime.h>
#include <hip/hip_bf16.h>
#include <cstdint>

#define GAS __attribute__((address_space(1)))
#define LAS __attribute__((address_space(3)))
typedef unsigned long long u64;
typedef unsigned short u16;
using u32x4 = __attribute__((ext_vector_type(4))) unsigned;
using u32x2 = __attribute__((ext_vector_type(2))) unsigned;
using f32x4 = __attribute__((ext_vector_type(4))) float;
using f32x2 = __attribute__((ext_vector_type(2))) float;
using bf16x2 = __attribute__((ext_vector_type(2))) __bf16;
using bf16x8 = __attribute__((ext_vector_type(8))) __bf16;
using s16x4 = __attribute__((ext_vector_type(4))) short;
typedef GAS u64 gu64;
#define RLX_AGENT __ATOMIC_RELAXED, __HIP_MEMORY_SCOPE_AGENT
#define RLX_WG __ATOMIC_RELAXED, __HIP_MEMORY_SCOPE_WORKGROUP

#define THR 12
#define PTHR 12
#define SCHED 39.0
#define D0 0.5
#define FREE 0
#define APACE 24
#define XT 3
#define ATHR 4
#define NPOLL 2
#define XMAP 0
#define ABL 0
#define STAMPS 0

constexpr int NB = 4, DM = 640, NH = 32, SK = 4096, DH = 128, FF = 1728;
constexpr int NWG = 256;
constexpr int WS_E = 0;
constexpr int WS_QX = 4096;
constexpr int WS_GX = WS_QX + 96 * 2048;
constexpr int WS_OX = WS_GX + 96 * 2048;
constexpr int WS_YX = WS_OX + 256 * 640;
constexpr int WS_ST = WS_YX + 160 * 2048;
constexpr int L_CTL = 0;
constexpr int L_Q = 64;
constexpr int L_H = L_Q + 256;
constexpr int L_O = L_H + 4 * 144;
constexpr int L_MRG = L_O + 4 * 272;
constexpr int L_WO = L_MRG + 4 * 132 * 4;
constexpr int L_WD = L_WO + 128 * 272;
constexpr int L_KV = L_WD + 64 * 272;
constexpr int L_END = L_KV + 4 * XT * 8192;
constexpr int L_RED = L_WO;
constexpr int L_XT = L_RED + 16384;
static_assert(L_O % 16 == 0 && L_H % 16 == 0 && L_MRG % 16 == 0 && L_WO % 16 == 0 && L_KV % 16 == 0 && L_END <= 163840, "lds");

struct Params { const u16* x; const u16* k; const u16* v; const u16* wq; const u16* wo; const u16* wg; const u16* wu; const u16* wd; const u16* gw; u16* out; unsigned char* ws; };

__device__ __forceinline__ float dot2(unsigned a, unsigned b, float c) {
  return __builtin_amdgcn_fdot2_f32_bf16(__builtin_bit_cast(bf16x2, a), __builtin_bit_cast(bf16x2, b), c, false);
}
template <int CTRL> __device__ __forceinline__ float dpp(float x) {
  return __builtin_bit_cast(float, __builtin_amdgcn_mov_dpp(__builtin_bit_cast(int, x), CTRL, 0xf, 0xf, true));
}
__device__ __forceinline__ float row_sum(float x) {
  x += dpp<0x128>(x); x += dpp<0x124>(x); x += dpp<0x122>(x); x += dpp<0x121>(x); return x;
}
__device__ __forceinline__ float xrow_max(float x) {
  auto s = __builtin_amdgcn_permlane16_swap(__float_as_uint(x), __float_as_uint(x), false, false);
  x = fmaxf(__uint_as_float(s[0]), __uint_as_float(s[1]));
  auto t = __builtin_amdgcn_permlane32_swap(__float_as_uint(x), __float_as_uint(x), false, false);
  return fmaxf(__uint_as_float(t[0]), __uint_as_float(t[1]));
}
__device__ __forceinline__ float xrow_sum(float x) {
  auto s = __builtin_amdgcn_permlane16_swap(__float_as_uint(x), __float_as_uint(x), false, false);
  x = __uint_as_float(s[0]) + __uint_as_float(s[1]);
  auto t = __builtin_amdgcn_permlane32_swap(__float_as_uint(x), __float_as_uint(x), false, false);
  return __uint_as_float(t[0]) + __uint_as_float(t[1]);
}
__device__ __forceinline__ unsigned pk_bf16(float lo, float hi) {
  bf16x2 r = {(__bf16)lo, (__bf16)hi}; return __builtin_bit_cast(unsigned, r);
}
__device__ __forceinline__ float bf_lo(unsigned p) { return __uint_as_float(p << 16); }
__device__ __forceinline__ float bf_hi(unsigned p) { return __uint_as_float(p & 0xffff0000u); }
__device__ __forceinline__ float rbf(float x) { return bf_lo(pk_bf16(x, 0.f) & 0xffffu); }
__device__ __forceinline__ u32x4 ldnt(const u16* p) { return __builtin_nontemporal_load(reinterpret_cast<const u32x4*>(p)); }
__device__ __forceinline__ unsigned ldnt32(const u16* p) { return __builtin_nontemporal_load(reinterpret_cast<const unsigned*>(p)); }
__device__ __forceinline__ void dma16(const u16* gsrc, unsigned lds_dst) {
  unsigned keep;
  asm volatile("s_mov_b32 %0, m0\n\ts_mov_b32 m0, %2\n\ts_nop 0\n\tglobal_load_lds_dwordx4 %1, off nt\n\ts_mov_b32 m0, %0" : "=&s"(keep) : "v"(gsrc), "s"(lds_dst) : "memory");
}
__device__ __forceinline__ bf16x8 tr_frag(LAS unsigned char* a0, LAS unsigned char* a1) {
  s16x4 lo = __builtin_amdgcn_ds_read_tr16_b64_v4i16((LAS s16x4*)a0), hi = __builtin_amdgcn_ds_read_tr16_b64_v4i16((LAS s16x4*)a1);
  return __builtin_bit_cast(bf16x8, __builtin_shufflevector(lo, hi, 0, 1, 2, 3, 4, 5, 6, 7));
}
__device__ __forceinline__ f32x4 mfma(bf16x8 a, bf16x8 b, f32x4 c) { return __builtin_amdgcn_mfma_f32_16x16x32_bf16(a, b, c, 0, 0, 0); }
__device__ __forceinline__ unsigned lds_ld(LAS unsigned* p) { return __hip_atomic_load(p, RLX_WG); }
__device__ __forceinline__ void lds_st(LAS unsigned* p, unsigned v) { __hip_atomic_store(p, v, RLX_WG); }
template <int SLEEP> __device__ __forceinline__ void lds_wait_ge(LAS unsigned* p, unsigned want) {
  _Pragma("nounroll") for (unsigned n = 0; n < (1u << 22); ++n) { if ((unsigned)__builtin_amdgcn_readfirstlane(lds_ld(p)) >= want) break; if (SLEEP) __builtin_amdgcn_s_sleep(SLEEP); }
  asm volatile("" ::: "memory");
}
__device__ __forceinline__ u32x4 gr_pack(f32x4 v, unsigned tlo, unsigned thi) {
  const unsigned a = (__float_as_uint(v[0]) + 0x200u) >> 10, b = (__float_as_uint(v[1]) + 0x200u) >> 10, c = (__float_as_uint(v[2]) + 0x200u) >> 10, d = (__float_as_uint(v[3]) + 0x200u) >> 10;
  return u32x4{a | (d << 22), b | ((d >> 10) << 22), c | ((d >> 20) << 22) | (tlo << 24), thi};
}
__device__ __forceinline__ bool gr_ok(u32x4 g, unsigned tlo, unsigned thi) { return g[3] == thi && (g[2] >> 24) == tlo; }
__device__ __forceinline__ f32x4 gr_val(u32x4 g) {
  return f32x4{__uint_as_float(g[0] << 10), __uint_as_float(g[1] << 10), __uint_as_float(g[2] << 10),
               __uint_as_float(((g[0] >> 22) << 10) | ((g[1] >> 22) << 20) | ((g[2] >> 22) << 30))};
}
constexpr unsigned TAU = (unsigned)(SCHED * 100 * 16 / 64 + 0.5);
#define PACE() do { due += TAU; _Pragma("nounroll") while ((int)((unsigned)__builtin_amdgcn_s_memrealtime() * 16u - due) < 0) __builtin_amdgcn_s_sleep(1); } while (0)
constexpr unsigned SPIN_CAP = 1u << 17;
#define WAIT_LGKM0() asm volatile("s_waitcnt lgkmcnt(0)" ::: "memory")
#define SB() __builtin_amdgcn_sched_barrier(0)
#define VMCNT(n) __builtin_amdgcn_s_waitcnt(0x0F70 | ((n) & 15) | (((n) >> 4) << 14))
#define STAMP(i) do {} while (0)
#define GST(v, off) __builtin_amdgcn_raw_buffer_store_b128(gr_pack((v), tlo, thi), rs, (off), 0, 16)
#define GLD(off) __builtin_amdgcn_raw_buffer_load_b128(rs, (off), 0, 16)

constexpr float LOG2E = 1.4426950408889634f;
constexpr float SCALE = 0.08838834764831845f;

__global__ void __launch_bounds__(512, 2) fused_kernel(Params p) {
  __shared__ __attribute__((aligned(16))) unsigned char smem[L_END];
  LAS unsigned char* lds = (LAS unsigned char*)smem;
  LAS unsigned* ctl = (LAS unsigned*)lds;
  const int lane = threadIdx.x & 63, wave = __builtin_amdgcn_readfirstlane(threadIdx.x >> 6);
  const int bid = blockIdx.x;
  const int b = bid >> 6, h = (bid >> 1) & 31, sp = bid & 1, mem = b * 2 + sp;
  const int gid = h * 8 + mem;
  const __amdgpu_buffer_rsrc_t rs = __builtin_amdgcn_make_buffer_rsrc(p.ws, 0, WS_ST, 0x00020000);

  if (wave < 4) {
    const size_t eoff = ((size_t)(b * NH + h) * SK + (sp * 4 + wave) * 16) * DH + lane * 8;
    const u16* kp = p.k + eoff; const u16* vp = p.v + eoff;
    constexpr int TSTR = 128 * DH;
    u32x4 kb[4][4], vb[4][4];
    if (wave == 0) STAMP(0);
    unsigned due = (unsigned)__builtin_amdgcn_s_memrealtime() * 16u - TAU + (unsigned)(int)(D0 * 1600);
    __builtin_amdgcn_s_barrier();
    PACE();
#pragma unroll
    for (int t = 0; t < 4; ++t) { VMCNT(PTHR - 1); kb[0][t] = ldnt(kp + t * 512); }
    PACE();
#pragma unroll
    for (int t = 0; t < 4; ++t) { VMCNT(PTHR - 1); vb[0][t] = ldnt(vp + t * 512); }
#pragma unroll
    for (int r = 1; r < 4; ++r) {
      PACE();
#pragma unroll
      for (int t = 0; t < 4; ++t) { VMCNT(PTHR - 1); kb[r][t] = ldnt(kp + r * TSTR + t * 512); }
      PACE();
#pragma unroll
      for (int t = 0; t < 4; ++t) { VMCNT(PTHR - 1); vb[r][t] = ldnt(vp + r * TSTR + t * 512); }
    }
    if (wave == 0) STAMP(7);
    const unsigned lkv = (unsigned)(size_t)(lds + L_KV) + wave * (XT * 8192);
#pragma unroll 1
    for (int r = 0; r < XT; ++r) {
      PACE();
#pragma unroll
      for (int t = 0; t < 4; ++t) { VMCNT(PTHR - 1); dma16(kp + (4 + r) * TSTR + t * 512, lkv + r * 8192 + t * 1024); }
      PACE();
#pragma unroll
      for (int t = 0; t < 4; ++t) { VMCNT(PTHR - 1); dma16(vp + (4 + r) * TSTR + t * 512, lkv + r * 8192 + 4096 + t * 1024); }
    }
    if (wave == 0) STAMP(1);
    lds_wait_ge<1>(ctl + 0, 1);
    if (wave == 0) STAMP(2);
    const u32x4 q = *(LAS u32x4*)(lds + L_Q + (lane & 15) * 16);
    float m = -INFINITY, l = 0.f, acc[8];
#pragma unroll
    for (int j = 0; j < 8; ++j) acc[j] = 0.f;
    kp += (4 + XT) * TSTR; vp += (4 + XT) * TSTR;
#define TILE(KB, VB, r, ISSUE) do { \
      float s[4]; \
      _Pragma("unroll") for (int t = 0; t < 4; ++t) s[t] = dot2(KB[t][3], q[3], dot2(KB[t][2], q[2], dot2(KB[t][1], q[1], dot2(KB[t][0], q[0], 0.f)))); \
      asm volatile("" : "+v"(s[0]), "+v"(s[1]), "+v"(s[2]), "+v"(s[3])); \
      if (ISSUE) { SB(); PACE(); _Pragma("unroll") for (int t = 0; t < 4; ++t) { VMCNT(THR - 1); KB[t] = ldnt(kp + r * TSTR + t * 512); } SB(); } \
      _Pragma("unroll") for (int t = 0; t < 4; ++t) s[t] = row_sum(s[t]); \
      { unsigned a = pk_bf16(s[0], s[1]), c = pk_bf16(s[2], s[3]); \
        a = pk_bf16(bf_lo(a) * SCALE, bf_hi(a) * SCALE); c = pk_bf16(bf_lo(c) * SCALE, bf_hi(c) * SCALE); \
        s[0] = bf_lo(a); s[1] = bf_hi(a); s[2] = bf_lo(c); s[3] = bf_hi(c); } \
      const float mn = fmaxf(fmaxf(fmaxf(s[0], s[1]), fmaxf(s[2], s[3])), m); \
      const float al = __builtin_amdgcn_exp2f((m - mn) * LOG2E); m = mn; \
      _Pragma("unroll") for (int t = 0; t < 4; ++t) s[t] = __builtin_amdgcn_exp2f((s[t] - mn) * LOG2E); \
      l = fmaf(l, al, (s[0] + s[1]) + (s[2] + s[3])); \
      _Pragma("unroll") for (int j = 0; j < 8; ++j) acc[j] *= al; \
      _Pragma("unroll") for (int t = 0; t < 4; ++t) { \
        _Pragma("unroll") for (int j = 0; j < 4; ++j) { acc[2 * j] = fmaf(s[t], bf_lo(VB[t][j]), acc[2 * j]); acc[2 * j + 1] = fmaf(s[t], bf_hi(VB[t][j]), acc[2 * j + 1]); } } \
      asm volatile("" : "+v"(acc[0]), "+v"(acc[1]), "+v"(acc[2]), "+v"(acc[3]), "+v"(acc[4]), "+v"(acc[5]), "+v"(acc[6]), "+v"(acc[7]), "+v"(l)); \
      SB(); if (ISSUE) { PACE(); _Pragma("unroll") for (int t = 0; t < 4; ++t) { VMCNT(THR - 1); VB[t] = ldnt(vp + r * TSTR + t * 512); } SB(); } \
    } while (0)
    constexpr int NREF = 28 - XT, NIT = NREF / 4, REM = NREF % 4;
#pragma unroll 1
    for (int it = 0; it < NIT; ++it) {
      TILE(kb[0], vb[0], 0, true); TILE(kb[1], vb[1], 1, true); TILE(kb[2], vb[2], 2, true); TILE(kb[3], vb[3], 3, true);
      kp += 4 * TSTR; vp += 4 * TSTR;
      if (it < XT) {
        u32x4 kl[4], vl[4];
        LAS unsigned char* src = lds + L_KV + wave * (XT * 8192) + it * 8192 + lane * 16;
#pragma unroll
        for (int t = 0; t < 4; ++t) { kl[t] = *(LAS u32x4*)(src + t * 1024); vl[t] = *(LAS u32x4*)(src + 4096 + t * 1024); }
        TILE(kl, vl, 0, false);
      }
    }
    TILE(kb[0], vb[0], 0, REM > 0);
    if (lane == 0) lds_st(ctl + 3, 1u);
    if (wave == 0) STAMP(3);
    TILE(kb[1], vb[1], 1, REM > 1); TILE(kb[2], vb[2], 2, REM > 2); TILE(kb[3], vb[3], 3, false);
    if (REM > 0) TILE(kb[0], vb[0], 0, false);
    if (REM > 1) TILE(kb[1], vb[1], 0, false);
    if (REM > 2) TILE(kb[2], vb[2], 0, false);
#undef TILE
    STAMP(16 + wave);
    const float M = xrow_max(m), f = __builtin_amdgcn_exp2f((m - M) * LOG2E);
    l = xrow_sum(l * f);
#pragma unroll
    for (int j = 0; j < 8; ++j) acc[j] = xrow_sum(acc[j] * f);
    LAS float* rec = (LAS float*)(lds + L_MRG) + wave * 132;
    if (lane < 16) {
      *(LAS f32x4*)(rec + 4 + lane * 8) = f32x4{acc[0], acc[1], acc[2], acc[3]};
      *(LAS f32x4*)(rec + 8 + lane * 8) = f32x4{acc[4], acc[5], acc[6], acc[7]};
      if (lane == 0) *(LAS f32x2*)rec = f32x2{M, l};
    }
    WAIT_LGKM0();
    unsigned old = 0;
    if (lane == 0) old = __hip_atomic_fetch_add(ctl + 1, 1u, RLX_WG);
    old = __builtin_amdgcn_readfirstlane(old);
    if (old != 3) return;
    asm volatile("" ::: "memory");
    STAMP(4);
    LAS float* mr = (LAS float*)(lds + L_MRG);
    float mw[4], lw[4]; f32x4 aw[4];
#pragma unroll
    for (int w = 0; w < 4; ++w) { f32x2 t = *(LAS f32x2*)(mr + w * 132); mw[w] = t[0]; lw[w] = t[1]; aw[w] = *(LAS f32x4*)(mr + w * 132 + 4 + 4 * (lane & 31)); }
    const unsigned tlo = ctl[4], thi = ctl[5];
    const float MM = fmaxf(fmaxf(mw[0], mw[1]), fmaxf(mw[2], mw[3]));
    float L = 0.f; f32x4 o = {0.f, 0.f, 0.f, 0.f};
#pragma unroll
    for (int w = 0; w < 4; ++w) { const float g = __builtin_amdgcn_exp2f((mw[w] - MM) * LOG2E); L = fmaf(g, lw[w], L); o += g * aw[w]; }
    if (lane == 32) o = f32x4{MM, L, 0.f, 0.f};
    if (lane <= 32) GST(o, WS_OX + gid * 640 + lane * 16);
    STAMP(5);
    return;
  }

  const int aw = wave - 4, g4 = lane >> 4, c16 = lane & 15;
  u64 E = 0;
  if (aw == 3) E = __hip_atomic_load((gu64*)(p.ws + WS_E), RLX_AGENT);
  unsigned tlo = 0, thi = 0;
#define GET_TAG() do { lds_wait_ge<0>(ctl + 6, 1); tlo = ctl[4]; thi = ctl[5]; } while (0)
#define GATHER_Q() do { \
    if (aw >= 4 - NPOLL) { \
      const int off = WS_QX + h * 6144 + b * 512 + (lane & 31) * 16; \
      u32x4 v0, v1; bool got = false; \
      for (int i = 3 - aw; i > 0; --i) __builtin_amdgcn_s_sleep(20); \
      _Pragma("nounroll") for (unsigned n = 0; n < SPIN_CAP; ++n) { \
        asm volatile("" ::: "memory"); \
        v0 = GLD(off + (lane >> 5) * 2048); v1 = GLD(off + 4096); \
        if (__all(gr_ok(v0, tlo, thi) && gr_ok(v1, tlo, thi))) { got = true; break; } \
        if (__builtin_amdgcn_readfirstlane(lds_ld(ctl + 0))) break; \
      } \
      if (got) { \
        f32x4 a = gr_val(v0); const f32x4 c = gr_val(v1); \
        _Pragma("unroll") for (int r = 0; r < 4; ++r) { \
          auto t = __builtin_amdgcn_permlane32_swap(__float_as_uint(a[r]), __float_as_uint(a[r]), false, false); \
          const float qv = (__uint_as_float(t[0]) + __uint_as_float(t[1])) + c[r]; \
          if (lane < 32) *(LAS u16*)(lds + L_Q + ((lane & 3) * 32 + r * 8 + (lane >> 2)) * 2) = (u16)pk_bf16(qv, 0.f); } \
        WAIT_LGKM0(); \
        if (lane == 0) lds_st(ctl + 0, 1u); \
        STAMP(11); \
      } else if (lane == 0) lds_st(ctl + 0, 1u); \
    } } while (0)

  if ((mem & 1) && mem > 1) {
    const int part = (mem - 3) >> 1;
    const int nl = part == 0 ? 14 : 13;
    const int r0 = (part == 0 ? 0 : 16 + part * 208) + aw * nl * 4;
    u32x4 w[14];
#pragma unroll
    for (int i = 0; i < 14; ++i) w[i] = ldnt(p.wq + (size_t)(r0 + min(i, nl - 1) * 4 + g4) * (NH * DH) + h * DH + c16 * 8);
    unsigned xr[4];
#pragma unroll
    for (int bb = 0; bb < 4; ++bb) xr[bb] = ldnt32(p.x + bb * DM + r0 + 2 * min(lane, nl * 2 - 1));
    if (aw == 0 && lane < 16) lds_st(ctl + lane, 0u);
    WAIT_LGKM0();
    __builtin_amdgcn_s_barrier();
    if (aw == 3 && lane == 0) { ctl[4] = (unsigned)(E + 1) & 0xffu; ctl[5] = (unsigned)((E + 1) >> 8); WAIT_LGKM0(); lds_st(ctl + 6, 1u); }
    LAS unsigned char* xt = lds + L_XT + aw * 448;
    if (lane < 28) {
      u32x4 t = {__builtin_amdgcn_perm(xr[1], xr[0], 0x05040100u), __builtin_amdgcn_perm(xr[3], xr[2], 0x05040100u),
                 __builtin_amdgcn_perm(xr[1], xr[0], 0x07060302u), __builtin_amdgcn_perm(xr[3], xr[2], 0x07060302u)};
      if (lane >= nl * 2) t = u32x4{0u, 0u, 0u, 0u};
      *(LAS u32x4*)(xt + lane * 16) = t;
    }
    WAIT_LGKM0();
    unsigned msk[4];
#pragma unroll
    for (int d = 0; d < 4; ++d) msk[d] = ((c16 & 7) >> 1) == d ? ((c16 & 1) ? 0xffff0000u : 0x0000ffffu) : 0u;
    const unsigned sel = (c16 & 8) ? 0x03020302u : 0x01000100u;
#pragma unroll 1
    for (int pass = 0; pass < 2; ++pass) {
      if (pass == 1) {
        const u16* src = (c16 < 8 ? p.wg : p.wu) + h * 64 + (c16 & 7) * 8;
#pragma unroll
        for (int i = 0; i < 14; ++i) { __builtin_amdgcn_s_sleep(APACE); VMCNT(ATHR - 1); w[i] = ldnt(src + (size_t)(r0 + min(i, nl - 1) * 4 + g4) * FF); }
      }
      f32x4 d0 = {0.f, 0.f, 0.f, 0.f}, d1 = {0.f, 0.f, 0.f, 0.f};
#pragma unroll
      for (int i = 0; i < 14; ++i) {
        const u32x2 xx = *(LAS u32x2*)(xt + (i * 4 + g4) * 8);
        const unsigned x0 = __builtin_amdgcn_perm(xx[0], xx[0], sel), x1 = __builtin_amdgcn_perm(xx[1], xx[1], sel);
        d0 = mfma(__builtin_bit_cast(bf16x8, w[i]), __builtin_bit_cast(bf16x8, u32x4{x0 & msk[0], x0 & msk[1], x0 & msk[2], x0 & msk[3]}), d0);
        d1 = mfma(__builtin_bit_cast(bf16x8, w[i]), __builtin_bit_cast(bf16x8, u32x4{x1 & msk[0], x1 & msk[1], x1 & msk[2], x1 & msk[3]}), d1);
      }
      LAS f32x4* red = (LAS f32x4*)(lds + L_RED + pass * 8192);
      red[(aw * 2 + 0) * 64 + lane] = d0; red[(aw * 2 + 1) * 64 + lane] = d1;
      WAIT_LGKM0();
      unsigned old = 0;
      if (lane == 0) old = __hip_atomic_fetch_add(ctl + (pass ? 12 : 7), 1u, RLX_WG);
      old = __builtin_amdgcn_readfirstlane(old);
      GET_TAG();
      if (old == 3) {
        d0 = (red[lane] + red[128 + lane]) + (red[256 + lane] + red[384 + lane]);
        d1 = (red[64 + lane] + red[192 + lane]) + (red[320 + lane] + red[448 + lane]);
        const int off = (pass ? WS_GX : WS_QX) + (h * 3 + part) * 2048 + (c16 >> 3) * 512 + ((c16 & 7) * 4 + g4) * 16;
        GST(d0, off); GST(d1, off + 1024);
        STAMP(8 + pass);
      }
      if (pass == 0) { GATHER_Q(); if (h >= 27 || (ABL & 4)) return; lds_wait_ge<4>(ctl + 0, 1); }
    }
    return;
  }

  const int cc = mem < 2 ? mem : (mem >> 1) + 1;
  if (aw == 0 && lane < 16) lds_st(ctl + lane, 0u);
  WAIT_LGKM0();
  __builtin_amdgcn_s_barrier();
  if (aw == 3 && lane == 0) { ctl[4] = (unsigned)(E + 1) & 0xffu; ctl[5] = (unsigned)((E + 1) >> 8); WAIT_LGKM0(); lds_st(ctl + 6, 1u); }
  GET_TAG();
  if (aw == 3) STAMP(6);
  GATHER_Q();
  lds_wait_ge<4>(ctl + 0, 1);
  if (ABL & 4) return;
  const unsigned trq = (lane & 15) >> 2, trp = lane & 3;
  {
    u32x4 wv[8];
#pragma unroll
    for (int i = 0; i < 8; ++i) { __builtin_amdgcn_s_sleep(APACE); VMCNT(ATHR - 1); wv[i] = ldnt(p.wo + (size_t)(h * DH + aw * 32 + i * 4 + g4) * DM + cc * 128 + c16 * 8); }
#pragma unroll
    for (int i = 0; i < 8; ++i) *(LAS u32x4*)(lds + L_WO + (aw * 32 + i * 4 + g4) * 272 + c16 * 16) = wv[i];
    if (h < 27) {
#pragma unroll
      for (int i = 0; i < 4; ++i) { __builtin_amdgcn_s_sleep(APACE); wv[i] = ldnt(p.wd + (size_t)(h * 64 + aw * 16 + i * 4 + g4) * DM + cc * 128 + c16 * 8); }
#pragma unroll
      for (int i = 0; i < 4; ++i) *(LAS u32x4*)(lds + L_WD + (aw * 16 + i * 4 + g4) * 272 + c16 * 16) = wv[i];
    }
  }
  if (aw == 1) {
    unsigned gwv[5], xg[4][5];
#pragma unroll
    for (int i = 0; i < 5; ++i) {
      gwv[i] = ldnt32(p.gw + i * 128 + lane * 2);
#pragma unroll
      for (int bb = 0; bb < 4; ++bb) xg[bb][i] = ldnt32(p.x + bb * DM + i * 128 + lane * 2);
    }
#pragma unroll
    for (int bb = 0; bb < 4; ++bb) {
      float s = 0.f;
#pragma unroll
      for (int i = 0; i < 5; ++i) { const unsigned pr = pk_bf16(bf_lo(xg[bb][i]) * bf_lo(gwv[i]), bf_hi(xg[bb][i]) * bf_hi(gwv[i])); s += bf_lo(pr) + bf_hi(pr); }
      s = rbf(xrow_sum(row_sum(s)));
      const float gt = rbf(1.f / (1.f + __expf(-s)));
      if (lane == 0) *(LAS float*)(lds + L_CTL + 32 + bb * 4) = gt;
    }
  } else if (aw == 3 && h < 27) {
    const int off = WS_GX + h * 6144 + lane * 16;
    u32x4 v[6];
    _Pragma("nounroll") for (unsigned n = 0; n < SPIN_CAP; ++n) {
      bool ok = true;
      asm volatile("" ::: "memory");
#pragma unroll
      for (int i = 0; i < 6; ++i) v[i] = GLD(off + i * 1024);
#pragma unroll
      for (int i = 0; i < 6; ++i) ok &= gr_ok(v[i], tlo, thi);
      if (__all(ok)) break;
      __builtin_amdgcn_s_sleep(32);
    }
#pragma unroll
    for (int u = 0; u < 2; ++u) {
      const f32x4 s = (gr_val(v[u]) + gr_val(v[2 + u])) + gr_val(v[4 + u]);
#pragma unroll
      for (int r = 0; r < 4; ++r) {
        const float a = rbf(s[r]), hv = a * dpp<0x4E>(a);
        if ((lane & 2) == 0) *(LAS u16*)(lds + L_H + (u * 2 + (lane >> 5)) * 144 + (((lane & 1) * 4 + r) * 8 + ((lane >> 2) & 7)) * 2) = (u16)pk_bf16(hv, 0.f);
      }
    }
    STAMP(10);
  }
  WAIT_LGKM0();
  if (lane == 0) __hip_atomic_fetch_add(ctl + 2, 1u, RLX_WG);
  lds_wait_ge<4>(ctl + 2, 4);
  bf16x8 bo[2][4];
  f32x4 accz[2] = {f32x4{0.f, 0.f, 0.f, 0.f}, f32x4{0.f, 0.f, 0.f, 0.f}};
  {
    LAS unsigned char* ta = lds + L_WO + (g4 * 8 + trq) * 272 + trp * 8 + aw * 64;
#pragma unroll
    for (int ks = 0; ks < 4; ++ks) { bo[0][ks] = tr_frag(ta + ks * 8704, ta + ks * 8704 + 1088); bo[1][ks] = tr_frag(ta + ks * 8704 + 32, ta + ks * 8704 + 1088 + 32); }
    if (h < 27) {
      LAS unsigned char* td = lds + L_WD + (g4 * 8 + trq) * 272 + trp * 8 + aw * 64;
#pragma unroll
      for (int ks = 0; ks < 2; ++ks) {
        const bf16x8 af = *(LAS bf16x8*)(lds + L_H + (lane & 3) * 144 + ks * 64 + g4 * 16);
        accz[0] = mfma(af, tr_frag(td + ks * 8704, td + ks * 8704 + 1088), accz[0]);
        accz[1] = mfma(af, tr_frag(td + ks * 8704 + 32, td + ks * 8704 + 1088 + 32), accz[1]);
      }
    }
  }
  f32x4 gate = *(LAS f32x4*)(lds + L_CTL + 32);
#pragma unroll
  for (int i = 0; i < 4; ++i) { const float og = rbf(1.f - gate[i]); accz[0][i] *= og; accz[1][i] *= og; }
  asm volatile("" : "+v"(accz[0]), "+v"(accz[1]), "+v"(gate));
  if (aw == 0) STAMP(12);
  lds_wait_ge<4>(ctl + 3, 1);
  {
    const int off = WS_OX + (h * 8 + aw * 2) * 640;
    u32x4 v0, v1, v2;
    _Pragma("nounroll") for (unsigned n = 0; n < SPIN_CAP * 4; ++n) {
      asm volatile("" ::: "memory");
      v0 = GLD(off + (lane >> 5) * 640 + (lane & 31) * 16); v1 = GLD(off + 512); v2 = GLD(off + 640 + 512);
      if (__all(gr_ok(v0, tlo, thi) && gr_ok(v1, tlo, thi) && gr_ok(v2, tlo, thi))) break;
    }
    const float m0 = __uint_as_float(v1[0] << 10), m1 = __uint_as_float(v2[0] << 10);
    const float MM = fmaxf(m0, m1), e0 = __builtin_amdgcn_exp2f((m0 - MM) * LOG2E), e1 = __builtin_amdgcn_exp2f((m1 - MM) * LOG2E);
    const float inv = __builtin_amdgcn_rcpf(fmaf(e0, __uint_as_float(v1[1] << 10), e1 * __uint_as_float(v2[1] << 10)));
    f32x4 o = gr_val(v0) * ((lane < 32 ? e0 : e1) * inv);
#pragma unroll
    for (int r = 0; r < 4; ++r) {
      auto t = __builtin_amdgcn_permlane32_swap(__float_as_uint(o[r]), __float_as_uint(o[r]), false, false);
      o[r] = __uint_as_float(t[0]) + __uint_as_float(t[1]);
    }
    if (lane < 32) *(LAS u32x2*)(lds + L_O + aw * 272 + lane * 8) = u32x2{pk_bf16(o[0], o[1]), pk_bf16(o[2], o[3])};
    WAIT_LGKM0();
    if (lane == 0) __hip_atomic_fetch_add(ctl + 2, 1u, RLX_WG);
  }
  STAMP(20 + aw);
  lds_wait_ge<0>(ctl + 2, 8);
  if (aw == 0) STAMP(13);
  {
    f32x4 d0 = {0.f, 0.f, 0.f, 0.f}, d1 = {0.f, 0.f, 0.f, 0.f};
#pragma unroll
    for (int ks = 0; ks < 4; ++ks) {
      const bf16x8 af = *(LAS bf16x8*)(lds + L_O + (lane & 3) * 272 + ks * 64 + g4 * 16);
      d0 = mfma(af, bo[0][ks], d0); d1 = mfma(af, bo[1][ks], d1);
    }
    const int off = WS_YX + (h * 5 + cc) * 2048 + aw * 512 + lane * 16;
    if (lane < 16) { GST(gate * d0 + accz[0], off); GST(gate * d1 + accz[1], off + 256); }
  }
  if (aw == 0) STAMP(14);
  if (aw != 3 || h >= 8) return;
  {
    const int off = WS_YX + (g4 * 5 + cc) * 2048 + h * 256 + c16 * 16;
    u32x4 v[8];
    _Pragma("nounroll") for (unsigned n = 0; n < SPIN_CAP * 4; ++n) {
      bool ok = true;
      asm volatile("" ::: "memory");
#pragma unroll
      for (int i = 0; i < 8; ++i) v[i] = GLD(off + i * 20 * 2048);
#pragma unroll
      for (int i = 0; i < 8; ++i) ok &= gr_ok(v[i], tlo, thi);
      if (__all(ok)) break;
    }
    f32x4 s = gr_val(v[0]);
#pragma unroll
    for (int i = 1; i < 8; ++i) s += gr_val(v[i]);
    const float s0 = xrow_sum(s[0]), s1 = xrow_sum(s[1]), s2 = xrow_sum(s[2]), s3 = xrow_sum(s[3]);
    const float r = g4 == 0 ? s0 : g4 == 1 ? s1 : g4 == 2 ? s2 : s3;
    p.out[g4 * DM + cc * 128 + h * 16 + c16] = (u16)(pk_bf16(r, 0.f) & 0xffffu);
    if (gid == 0 && lane == 0) __hip_atomic_store((gu64*)(p.ws + WS_E), E + 1, RLX_AGENT);
    STAMP(15);
  }
}

extern "C" void kernel_launch(void* const* d_in, const int* in_sizes, int n_in, void* d_out, int out_size, void* d_ws, size_t ws_size,
                              hipStream_t stream) {
  Params p;
  p.x = (const u16*)d_in[0]; p.k = (const u16*)d_in[1]; p.v = (const u16*)d_in[2]; p.wq = (const u16*)d_in[3]; p.wo = (const u16*)d_in[4];
  p.wg = (const u16*)d_in[6]; p.wu = (const u16*)d_in[7]; p.wd = (const u16*)d_in[8]; p.gw = (const u16*)d_in[9];
  p.out = (u16*)d_out; p.ws = (unsigned char*)d_ws;
  hipLaunchKernelGGL(fused_kernel, dim3(NWG), dim3(512), 0, stream, p);
}
```

```cpp
#include <hip/hip_runtime.h>
#include <hip/hip_bf16.h>
#include <cstdint>

#define GAS __attribute__((address_space(1)))
#define LAS __attribute__((address_space(3)))
typedef unsigned long long u64;
typedef unsigned short u16;
using u32x4 = __attribute__((ext_vector_type(4))) unsigned;
using u32x2 = __attribute__((ext_vector_type(2))) unsigned;
using f32x4 = __attribute__((ext_vector_type(4))) float;
using f32x2 = __attribute__((ext_vector_type(2))) float;
using bf16x2 = __attribute__((ext_vector_type(2))) __bf16;
using bf16x8 = __attribute__((ext_vector_type(8))) __bf16;
using s16x4 = __attribute__((ext_vector_type(4))) short;
typedef GAS u64 gu64;
#define RLX_AGENT __ATOMIC_RELAXED, __HIP_MEMORY_SCOPE_AGENT
#define RLX_WG __ATOMIC_RELAXED, __HIP_MEMORY_SCOPE_WORKGROUP

#define SCHED 38.25
#define THR 12
#define APACE 24
#define QDELAY 60
#define RSTAG 6
#define STAMPS 0

constexpr int DM = 640, NH = 32, SK = 4096, DH = 128, FF = 1728;
constexpr int NWG = 256;
constexpr int WS_E = 0;
constexpr int WS_QX = 4096;
constexpr int WS_GX = WS_QX + 256 * 2048;
constexpr int WS_OX = WS_GX + 96 * 2048;
constexpr int WS_YX = WS_OX + 256 * 640;
constexpr int WS_END = WS_YX + 160 * 2048;
constexpr int WS_ST = WS_END;
constexpr int L_CTL = 0;
constexpr int L_Q = 64;
constexpr int L_H = L_Q + 256;
constexpr int L_O = L_H + 4 * 144;
constexpr int L_MRG = L_O + 4 * 272;
constexpr int L_RED = L_MRG + 4 * 132 * 4;
constexpr int L_XT = L_RED + 16384;
constexpr int L_KV = L_XT + 4 * 448;
constexpr int L_WO = L_KV;
constexpr int L_WD = L_WO + 128 * 272;
constexpr int L_END = L_KV + 4 * 32768;
static_assert(L_O % 16 == 0 && L_H % 16 == 0 && L_MRG % 16 == 0 && L_RED % 16 == 0 && L_KV % 16 == 0 && L_END <= 163840, "lds");

struct Params { const u16* x; const u16* k; const u16* v; const u16* wq; const u16* wo; const u16* wg; const u16* wu; const u16* wd; const u16* gw; u16* out; unsigned char* ws; };

__device__ __forceinline__ float dot2(unsigned a, unsigned b, float c) {
  return __builtin_amdgcn_fdot2_f32_bf16(__builtin_bit_cast(bf16x2, a), __builtin_bit_cast(bf16x2, b), c, false);
}
template <int CTRL> __device__ __forceinline__ float dpp(float x) {
  return __builtin_bit_cast(float, __builtin_amdgcn_mov_dpp(__builtin_bit_cast(int, x), CTRL, 0xf, 0xf, true));
}
__device__ __forceinline__ float row_sum(float x) {
  x += dpp<0x128>(x); x += dpp<0x124>(x); x += dpp<0x122>(x); x += dpp<0x121>(x); return x;
}
__device__ __forceinline__ float half_sum(float x) {
  auto t = __builtin_amdgcn_permlane32_swap(__float_as_uint(x), __float_as_uint(x), false, false);
  return __uint_as_float(t[0]) + __uint_as_float(t[1]);
}
__device__ __forceinline__ float xrow_max(float x) {
  auto s = __builtin_amdgcn_permlane16_swap(__float_as_uint(x), __float_as_uint(x), false, false);
  x = fmaxf(__uint_as_float(s[0]), __uint_as_float(s[1]));
  auto t = __builtin_amdgcn_permlane32_swap(__float_as_uint(x), __float_as_uint(x), false, false);
  return fmaxf(__uint_as_float(t[0]), __uint_as_float(t[1]));
}
__device__ __forceinline__ float xrow_sum(float x) {
  auto s = __builtin_amdgcn_permlane16_swap(__float_as_uint(x), __float_as_uint(x), false, false);
  return half_sum(__uint_as_float(s[0]) + __uint_as_float(s[1]));
}
__device__ __forceinline__ unsigned pk_bf16(float lo, float hi) {
  bf16x2 r = {(__bf16)lo, (__bf16)hi}; return __builtin_bit_cast(unsigned, r);
}
__device__ __forceinline__ float bf_lo(unsigned p) { return __uint_as_float(p << 16); }
__device__ __forceinline__ float bf_hi(unsigned p) { return __uint_as_float(p & 0xffff0000u); }
__device__ __forceinline__ float rbf(float x) { return bf_lo(pk_bf16(x, 0.f) & 0xffffu); }
__device__ __forceinline__ u32x4 ldnt(const u16* p) { return __builtin_nontemporal_load(reinterpret_cast<const u32x4*>(p)); }
__device__ __forceinline__ unsigned ldnt32(const u16* p) { return __builtin_nontemporal_load(reinterpret_cast<const unsigned*>(p)); }
__device__ __forceinline__ void dma16(const u16* gsrc, unsigned lds_dst) {
  unsigned keep;
  asm volatile("s_mov_b32 %0, m0\n\ts_mov_b32 m0, %2\n\ts_nop 0\n\tglobal_load_lds_dwordx4 %1, off nt\n\ts_mov_b32 m0, %0" : "=&s"(keep) : "v"(gsrc), "s"(lds_dst) : "memory");
}
__device__ __forceinline__ bf16x8 tr_frag(LAS unsigned char* a0, LAS unsigned char* a1) {
  s16x4 lo = __builtin_amdgcn_ds_read_tr16_b64_v4i16((LAS s16x4*)a0), hi = __builtin_amdgcn_ds_read_tr16_b64_v4i16((LAS s16x4*)a1);
  return __builtin_bit_cast(bf16x8, __builtin_shufflevector(lo, hi, 0, 1, 2, 3, 4, 5, 6, 7));
}
__device__ __forceinline__ f32x4 mfma(bf16x8 a, bf16x8 b, f32x4 c) { return __builtin_amdgcn_mfma_f32_16x16x32_bf16(a, b, c, 0, 0, 0); }
__device__ __forceinline__ unsigned lds_ld(LAS unsigned* p) { return __hip_atomic_load(p, RLX_WG); }
__device__ __forceinline__ void lds_st(LAS unsigned* p, unsigned v) { __hip_atomic_store(p, v, RLX_WG); }
template <int SLEEP> __device__ __forceinline__ void lds_wait_ge(LAS unsigned* p, unsigned want) {
  _Pragma("nounroll") for (unsigned n = 0; n < (1u << 21); ++n) { if ((unsigned)__builtin_amdgcn_readfirstlane(lds_ld(p)) >= want) break; if (SLEEP) __builtin_amdgcn_s_sleep(SLEEP); }
  asm volatile("" ::: "memory");
}
__device__ __forceinline__ u32x4 gr_pack(f32x4 v, unsigned ta, unsigned tb) {
  const unsigned a = (__float_as_uint(v[0]) + 0x200u) >> 10, b = (__float_as_uint(v[1]) + 0x200u) >> 10, c = (__float_as_uint(v[2]) + 0x200u) >> 10, d = (__float_as_uint(v[3]) + 0x200u) >> 10;
  return u32x4{a | (b << 22), (b >> 10) | ta, c | (d << 22), (d >> 10) | tb};
}
__device__ __forceinline__ bool gr_ok(u32x4 g, unsigned ta, unsigned tb) { return ((g[1] ^ ta) | (g[3] ^ tb)) < 4096u; }
__device__ __forceinline__ f32x4 gr_val(u32x4 g) {
  return f32x4{__uint_as_float(g[0] << 10), __uint_as_float(((g[0] >> 22) << 10) | (g[1] << 20)), __uint_as_float(g[2] << 10), __uint_as_float(((g[2] >> 22) << 10) | (g[3] << 20))};
}
constexpr unsigned SPIN_CAP = 1u << 17;
#define WAIT_LGKM0() asm volatile("s_waitcnt lgkmcnt(0)" ::: "memory")
#define SB() __builtin_amdgcn_sched_barrier(0)
#define VMCNT(n) __builtin_amdgcn_s_waitcnt(0x0F70 | ((n) & 15) | (((n) >> 4) << 14))
#define NOW16() ((unsigned)__builtin_amdgcn_s_memrealtime() * 16u)
#define PACE() do { due += tau; _Pragma("nounroll") while ((int)(NOW16() - due) < 0) __builtin_amdgcn_s_sleep(1); } while (0)
#define GST(v, off) __builtin_amdgcn_raw_buffer_store_b128(gr_pack((v), ta, tb), rs, (off), 0, 16)
#define GLD(off) __builtin_amdgcn_raw_buffer_load_b128(rs, (off), 0, 16)
#define STAMP(i) do {} while (0)

constexpr float LOG2E = 1.4426950408889634f;
constexpr float SCALE = 0.08838834764831845f;

__global__ void __launch_bounds__(512, 2) fused_kernel(Params p) {
  __shared__ __attribute__((aligned(16))) unsigned char smem[L_END];
  LAS unsigned char* lds = (LAS unsigned char*)smem;
  LAS unsigned* ctl = (LAS unsigned*)lds;
  const int lane = threadIdx.x & 63, wave = __builtin_amdgcn_readfirstlane(threadIdx.x >> 6);
  const int bid = blockIdx.x;
  const int b = bid >> 6, h = (bid >> 1) & 31, sp = bid & 1, mem = b * 2 + sp, gid = h * 8 + mem;
  const __amdgpu_buffer_rsrc_t rs = __builtin_amdgcn_make_buffer_rsrc(p.ws, 0, WS_END, 0x00020000);

  if (__builtin_expect(wave < 4, 1)) {
    const int nx = sp ? 0 : 2;
    const unsigned tau = sp ? (unsigned)(SCHED * 800 / 31) : (unsigned)(SCHED * 800 / 33);
    unsigned due = NOW16() - tau;
    const size_t eoff = ((size_t)(b * NH + h) * SK + (sp * 4 + wave) * 16) * DH + lane * 8;
    const u16* kp = p.k + eoff; const u16* vp = p.v + eoff;
    constexpr int TSTR = 128 * DH;
    u32x4 kb[4][4], vb[4][4];
    if (wave == 0) STAMP(0);
#pragma unroll
    for (int r = 0; r < 4; ++r) {
      PACE();
#pragma unroll
      for (int t = 0; t < 4; ++t) { VMCNT(THR - 1); kb[r][t] = ldnt(kp + r * TSTR + t * 512); }
      if (r == 0) __builtin_amdgcn_s_barrier();
      PACE();
#pragma unroll
      for (int t = 0; t < 4; ++t) { VMCNT(THR - 1); vb[r][t] = ldnt(vp + r * TSTR + t * 512); }
    }
    const unsigned lkv = (unsigned)(size_t)(lds + L_KV) + wave * 32768;
#define PARK(slot, eo) do { \
      PACE(); \
      _Pragma("unroll") for (int t = 0; t < 4; ++t) { VMCNT(THR - 1); dma16(kp + (eo) + t * 512, lkv + (slot) * 8192 + t * 1024); } \
      PACE(); \
      _Pragma("unroll") for (int t = 0; t < 4; ++t) { VMCNT(THR - 1); dma16(vp + (eo) + t * 512, lkv + (slot) * 8192 + 4096 + t * 1024); } \
    } while (0)
#pragma unroll 1
    for (int r = 0; r < (nx ? 4 : 3); ++r) PARK(r, r < 3 ? (4 + r) * TSTR : 31 * TSTR);
    if (wave == 0) STAMP(1);
    lds_wait_ge<1>(ctl + 0, 1);
    const u32x4 q = *(LAS u32x4*)(lds + L_Q + (lane & 15) * 16);
    float m = -INFINITY, l = 0.f, acc[8];
#pragma unroll
    for (int j = 0; j < 8; ++j) acc[j] = 0.f;
    kp += 7 * TSTR; vp += 7 * TSTR;
#define TILE(KB, VB, r, ISSUE) do { \
      float s[4]; \
      _Pragma("unroll") for (int t = 0; t < 4; ++t) s[t] = dot2(KB[t][3], q[3], dot2(KB[t][2], q[2], dot2(KB[t][1], q[1], dot2(KB[t][0], q[0], 0.f)))); \
      asm volatile("" : "+v"(s[0]), "+v"(s[1]), "+v"(s[2]), "+v"(s[3])); \
      if (ISSUE) { SB(); PACE(); _Pragma("unroll") for (int t = 0; t < 4; ++t) { VMCNT(THR - 1); KB[t] = ldnt(kp + r * TSTR + t * 512); } SB(); } \
      _Pragma("unroll") for (int t = 0; t < 4; ++t) s[t] = row_sum(s[t]); \
      { unsigned a = pk_bf16(s[0], s[1]), c = pk_bf16(s[2], s[3]); \
        a = pk_bf16(bf_lo(a) * SCALE, bf_hi(a) * SCALE); c = pk_bf16(bf_lo(c) * SCALE, bf_hi(c) * SCALE); \
        s[0] = bf_lo(a); s[1] = bf_hi(a); s[2] = bf_lo(c); s[3] = bf_hi(c); } \
      const float mn = fmaxf(fmaxf(fmaxf(s[0], s[1]), fmaxf(s[2], s[3])), m); \
      const float al = __builtin_amdgcn_exp2f((m - mn) * LOG2E); m = mn; \
      _Pragma("unroll") for (int t = 0; t < 4; ++t) s[t] = __builtin_amdgcn_exp2f((s[t] - mn) * LOG2E); \
      l = fmaf(l, al, (s[0] + s[1]) + (s[2] + s[3])); \
      _Pragma("unroll") for (int j = 0; j < 8; ++j) acc[j] *= al; \
      _Pragma("unroll") for (int t = 0; t < 4; ++t) { \
        _Pragma("unroll") for (int j = 0; j < 4; ++j) { acc[2 * j] = fmaf(s[t], bf_lo(VB[t][j]), acc[2 * j]); acc[2 * j + 1] = fmaf(s[t], bf_hi(VB[t][j]), acc[2 * j + 1]); } } \
      asm volatile("" : "+v"(acc[0]), "+v"(acc[1]), "+v"(acc[2]), "+v"(acc[3]), "+v"(acc[4]), "+v"(acc[5]), "+v"(acc[6]), "+v"(acc[7]), "+v"(l)); \
      SB(); if (ISSUE) { PACE(); _Pragma("unroll") for (int t = 0; t < 4; ++t) { VMCNT(THR - 1); VB[t] = ldnt(vp + r * TSTR + t * 512); } SB(); } \
    } while (0)
#pragma unroll 1
    for (int it = 0; it < 6; ++it) {
      TILE(kb[0], vb[0], 0, true); TILE(kb[1], vb[1], 1, true); TILE(kb[2], vb[2], 2, true); TILE(kb[3], vb[3], 3, true);
      kp += 4 * TSTR; vp += 4 * TSTR;
      if (it < 3 + nx) {
        u32x4 kl[4], vl[4];
        LAS unsigned char* src = lds + L_KV + wave * 32768 + (it & 3) * 8192 + lane * 16;
#pragma unroll
        for (int t = 0; t < 4; ++t) { kl[t] = *(LAS u32x4*)(src + t * 1024); vl[t] = *(LAS u32x4*)(src + 4096 + t * 1024); }
        TILE(kl, vl, 0, false);
        if (it == 2 + nx && lane == 0) __hip_atomic_fetch_add(ctl + 13, 1u, RLX_WG);
        if (it == 1 && nx == 2) PARK(0, 16 * TSTR + 64 * DH);
      }
    }
    if (lane == 0) lds_st(ctl + 14, 1u);
    TILE(kb[0], vb[0], 0, false); TILE(kb[1], vb[1], 1, false); TILE(kb[2], vb[2], 2, false); TILE(kb[3], vb[3], 3, false);
#undef TILE
    STAMP(16 + wave);
    const float M = xrow_max(m), f = __builtin_amdgcn_exp2f((m - M) * LOG2E);
    l = xrow_sum(l * f);
#pragma unroll
    for (int j = 0; j < 8; ++j) acc[j] = xrow_sum(acc[j] * f);
    LAS float* rec = (LAS float*)(lds + L_MRG) + wave * 132;
    if (lane < 16) {
      *(LAS f32x4*)(rec + 4 + lane * 8) = f32x4{acc[0], acc[1], acc[2], acc[3]};
      *(LAS f32x4*)(rec + 8 + lane * 8) = f32x4{acc[4], acc[5], acc[6], acc[7]};
      if (lane == 0) *(LAS f32x2*)rec = f32x2{M, l};
    }
    WAIT_LGKM0();
    unsigned old = 0;
    if (lane == 0) old = __hip_atomic_fetch_add(ctl + 1, 1u, RLX_WG);
    old = __builtin_amdgcn_readfirstlane(old);
    if (old != 3) return;
    asm volatile("" ::: "memory");
    LAS float* mr = (LAS float*)(lds + L_MRG);
    float mw[4], lw[4]; f32x4 av[4];
#pragma unroll
    for (int w = 0; w < 4; ++w) { f32x2 t = *(LAS f32x2*)(mr + w * 132); mw[w] = t[0]; lw[w] = t[1]; av[w] = *(LAS f32x4*)(mr + w * 132 + 4 + 4 * (lane & 31)); }
    const unsigned ta = ctl[4], tb = ctl[5];
    const float MM = fmaxf(fmaxf(mw[0], mw[1]), fmaxf(mw[2], mw[3]));
    float L = 0.f; f32x4 o = {0.f, 0.f, 0.f, 0.f};
#pragma unroll
    for (int w = 0; w < 4; ++w) { const float g = __builtin_amdgcn_exp2f((mw[w] - MM) * LOG2E); L = fmaf(g, lw[w], L); o += g * av[w]; }
    if (lane == 32) o = f32x4{MM, 0.f, L, 0.f};
    if (lane <= 32) GST(o, WS_OX + gid * 640 + lane * 16);
    STAMP(5);
    return;
  }

  const int aw = wave - 4, g4 = lane >> 4, c16 = lane & 15;
  u64 E = 0;
  unsigned ta = 0, tb = 0;
  unsigned msk[4];
#pragma unroll
  for (int d = 0; d < 4; ++d) msk[d] = ((c16 & 7) >> 1) == d ? ((c16 & 1) ? 0xffff0000u : 0x0000ffffu) : 0u;
  const unsigned sel = (c16 & 8) ? 0x03020302u : 0x01000100u;
  LAS unsigned char* xt = lds + L_XT + aw * 448;
#define PROJECT(NL, W, REDOFF, CNT, DSTOFF, ST) do { \
    f32x4 d0 = {0.f, 0.f, 0.f, 0.f}, d1 = {0.f, 0.f, 0.f, 0.f}; \
    _Pragma("unroll") for (int i = 0; i < NL; ++i) { \
      const u32x2 xx = *(LAS u32x2*)(xt + (i * 4 + g4) * 8); \
      const unsigned x0 = __builtin_amdgcn_perm(xx[0], xx[0], sel), x1 = __builtin_amdgcn_perm(xx[1], xx[1], sel); \
      d0 = mfma(__builtin_bit_cast(bf16x8, W[i]), __builtin_bit_cast(bf16x8, u32x4{x0 & msk[0], x0 & msk[1], x0 & msk[2], x0 & msk[3]}), d0); \
      d1 = mfma(__builtin_bit_cast(bf16x8, W[i]), __builtin_bit_cast(bf16x8, u32x4{x1 & msk[0], x1 & msk[1], x1 & msk[2], x1 & msk[3]}), d1); \
    } \
    LAS f32x4* red = (LAS f32x4*)(lds + L_RED + REDOFF); \
    red[(aw * 2 + 0) * 64 + lane] = d0; red[(aw * 2 + 1) * 64 + lane] = d1; \
    WAIT_LGKM0(); \
    unsigned old = 0; \
    if (lane == 0) old = __hip_atomic_fetch_add(ctl + CNT, 1u, RLX_WG); \
    old = __builtin_amdgcn_readfirstlane(old); \
    lds_wait_ge<0>(ctl + 6, 1); ta = ctl[4]; tb = ctl[5]; \
    if (old == 3) {     \
      d0 = (red[lane] + red[128 + lane]) + (red[256 + lane] + red[384 + lane]); \
      d1 = (red[64 + lane] + red[192 + lane]) + (red[320 + lane] + red[448 + lane]); \
      const int off = (DSTOFF) + (c16 >> 3) * 512 + ((c16 & 7) * 4 + g4) * 16; \
      GST(d0, off); GST(d1, off + 1024); \
      STAMP(ST); \
    } } while (0)
#define XTABLE(NROWS2) do { \
    u32x4 t = {__builtin_amdgcn_perm(xr[1], xr[0], 0x05040100u), __builtin_amdgcn_perm(xr[3], xr[2], 0x05040100u), \
               __builtin_amdgcn_perm(xr[1], xr[0], 0x07060302u), __builtin_amdgcn_perm(xr[3], xr[2], 0x07060302u)}; \
    if (lane >= (NROWS2)) t = u32x4{0u, 0u, 0u, 0u}; \
    if (lane < 28) *(LAS u32x4*)(xt + lane * 16) = t; \
    WAIT_LGKM0(); } while (0)
  {
    const int r0 = mem * 80 + aw * 20;
    u32x4 w[5]; unsigned xr[4];
#pragma unroll
    for (int i = 0; i < 5; ++i) w[i] = ldnt(p.wq + (size_t)(r0 + i * 4 + g4) * (NH * DH) + h * DH + c16 * 8);
#pragma unroll
    for (int bb = 0; bb < 4; ++bb) xr[bb] = ldnt32(p.x + bb * DM + r0 + 2 * min(lane, 9));
    if (aw == 3) E = __hip_atomic_load((gu64*)(p.ws + WS_E), RLX_AGENT);
    if (aw == 0 && lane < 16) lds_st(ctl + lane, 0u);
    WAIT_LGKM0();
    __builtin_amdgcn_s_barrier();
    XTABLE(10);
    asm volatile("" : "+v"(E));
    E += 1;
    if (aw == 3 && lane == 0) { ctl[4] = (unsigned)E << 12; ctl[5] = ((unsigned)(E >> 20) + (unsigned)E * 0x9E5u) << 12; WAIT_LGKM0(); lds_st(ctl + 6, 1u); }
    PROJECT(5, w, 0, 7, WS_QX + gid * 2048, 8);
    if (aw == 3) {
      const int off = WS_QX + h * 16384 + (lane >> 5) * 2048 + b * 512 + (lane & 31) * 16;
      u32x4 v[4];
      __builtin_amdgcn_s_sleep(QDELAY);
      _Pragma("nounroll") for (unsigned n = 0; n < SPIN_CAP; ++n) {
        asm volatile("" ::: "memory");
#pragma unroll
        for (int i = 0; i < 4; ++i) v[i] = GLD(off + i * 4096);
        if (__all(gr_ok(v[0], ta, tb) && gr_ok(v[1], ta, tb) && gr_ok(v[2], ta, tb) && gr_ok(v[3], ta, tb))) break;
      }
      const f32x4 a = (gr_val(v[0]) + gr_val(v[1])) + (gr_val(v[2]) + gr_val(v[3]));
#pragma unroll
      for (int r = 0; r < 4; ++r) {
        const float qv = half_sum(a[r]);
        if (lane < 32) *(LAS u16*)(lds + L_Q + ((lane & 3) * 32 + r * 8 + (lane >> 2)) * 2) = (u16)pk_bf16(qv, 0.f);
      }
      WAIT_LGKM0();
      if (lane == 0) lds_st(ctl + 0, 1u);
      STAMP(11);
    }
    lds_wait_ge<4>(ctl + 0, 1);
  }
  if ((mem & 1) && mem > 1) {
    if (h >= 27) return;
    const int part = (mem - 3) >> 1, nl = part == 0 ? 14 : 13;
    const int r0 = (part == 0 ? 0 : 16 + part * 208) + aw * nl * 4;
    u32x4 w[14]; unsigned xr[4];
#pragma unroll
    for (int bb = 0; bb < 4; ++bb) xr[bb] = ldnt32(p.x + bb * DM + r0 + 2 * min(lane, nl * 2 - 1));
    const u16* src = (c16 < 8 ? p.wg : p.wu) + h * 64 + (c16 & 7) * 8;
#pragma unroll
    for (int i = 0; i < 14; ++i) { __builtin_amdgcn_s_sleep(APACE); VMCNT(3); w[i] = ldnt(src + (size_t)(r0 + min(i, nl - 1) * 4 + g4) * FF); }
    XTABLE(nl * 2);
    PROJECT(14, w, 8192, 12, WS_GX + (h * 3 + part) * 2048, 9);
    return;
  }

  const int cc = mem < 2 ? mem : (mem >> 1) + 1;
  const unsigned trq = (lane & 15) >> 2, trp = lane & 3;
  if (aw == 1) {
    unsigned gwv[5], xg[4][5];
#pragma unroll
    for (int i = 0; i < 5; ++i) {
      gwv[i] = ldnt32(p.gw + i * 128 + lane * 2);
#pragma unroll
      for (int bb = 0; bb < 4; ++bb) xg[bb][i] = ldnt32(p.x + bb * DM + i * 128 + lane * 2);
    }
#pragma unroll
    for (int bb = 0; bb < 4; ++bb) {
      float s = 0.f;
#pragma unroll
      for (int i = 0; i < 5; ++i) { const unsigned pr = pk_bf16(bf_lo(xg[bb][i]) * bf_lo(gwv[i]), bf_hi(xg[bb][i]) * bf_hi(gwv[i])); s += bf_lo(pr) + bf_hi(pr); }
      s = rbf(xrow_sum(row_sum(s)));
      const float gt = rbf(1.f / (1.f + __expf(-s)));
      if (lane == 0) *(LAS float*)(lds + L_CTL + 32 + bb * 4) = gt;
    }
  } else if (aw == 3 && h < 27) {
    const int off = WS_GX + h * 6144 + lane * 16;
    u32x4 v[6];
    _Pragma("nounroll") for (unsigned n = 0; n < SPIN_CAP; ++n) {
      bool ok = true;
      __builtin_amdgcn_s_sleep(64);
      asm volatile("" ::: "memory");
#pragma unroll
      for (int i = 0; i < 6; ++i) v[i] = GLD(off + i * 1024);
#pragma unroll
      for (int i = 0; i < 6; ++i) ok &= gr_ok(v[i], ta, tb);
      if (__all(ok)) break;
    }
#pragma unroll
    for (int u = 0; u < 2; ++u) {
      const f32x4 s = (gr_val(v[u]) + gr_val(v[2 + u])) + gr_val(v[4 + u]);
#pragma unroll
      for (int r = 0; r < 4; ++r) {
        const float a = rbf(s[r]), hv = a * dpp<0x4E>(a);
        if ((lane & 2) == 0) *(LAS u16*)(lds + L_H + (u * 2 + (lane >> 5)) * 144 + (((lane & 1) * 4 + r) * 8 + ((lane >> 2) & 7)) * 2) = (u16)pk_bf16(hv, 0.f);
      }
    }
    STAMP(10);
  }
  {
    lds_wait_ge<4>(ctl + 14, 1);
    u32x4 wv[8], wd[4];
#pragma unroll
    for (int i = 0; i < 8; ++i) wv[i] = ldnt(p.wo + (size_t)(h * DH + aw * 32 + i * 4 + g4) * DM + cc * 128 + c16 * 8);
    if (h < 27) {
#pragma unroll
      for (int i = 0; i < 4; ++i) wd[i] = ldnt(p.wd + (size_t)(h * 64 + aw * 16 + i * 4 + g4) * DM + cc * 128 + c16 * 8);
    }
    lds_wait_ge<1>(ctl + 13, 4);
#pragma unroll
    for (int i = 0; i < 8; ++i) *(LAS u32x4*)(lds + L_WO + (aw * 32 + i * 4 + g4) * 272 + c16 * 16) = wv[i];
    if (h < 27) {
#pragma unroll
      for (int i = 0; i < 4; ++i) *(LAS u32x4*)(lds + L_WD + (aw * 16 + i * 4 + g4) * 272 + c16 * 16) = wd[i];
    }
  }
  WAIT_LGKM0();
  if (lane == 0) __hip_atomic_fetch_add(ctl + 2, 1u, RLX_WG);
  lds_wait_ge<0>(ctl + 2, 4);
  bf16x8 bo[2][4];
  f32x4 accz[2] = {f32x4{0.f, 0.f, 0.f, 0.f}, f32x4{0.f, 0.f, 0.f, 0.f}};
  {
    LAS unsigned char* tw = lds + L_WO + (g4 * 8 + trq) * 272 + trp * 8 + aw * 64;
#pragma unroll
    for (int ks = 0; ks < 4; ++ks) { bo[0][ks] = tr_frag(tw + ks * 8704, tw + ks * 8704 + 1088); bo[1][ks] = tr_frag(tw + ks * 8704 + 32, tw + ks * 8704 + 1088 + 32); }
    if (h < 27) {
      LAS unsigned char* td = lds + L_WD + (g4 * 8 + trq) * 272 + trp * 8 + aw * 64;
#pragma unroll
      for (int ks = 0; ks < 2; ++ks) {
        const bf16x8 af = *(LAS bf16x8*)(lds + L_H + (lane & 3) * 144 + ks * 64 + g4 * 16);
        accz[0] = mfma(af, tr_frag(td + ks * 8704, td + ks * 8704 + 1088), accz[0]);
        accz[1] = mfma(af, tr_frag(td + ks * 8704 + 32, td + ks * 8704 + 1088 + 32), accz[1]);
      }
    }
  }
  f32x4 gate = *(LAS f32x4*)(lds + L_CTL + 32);
#pragma unroll
  for (int i = 0; i < 4; ++i) { const float og = rbf(1.f - gate[i]); accz[0][i] *= og; accz[1][i] *= og; }
  asm volatile("" : "+v"(accz[0]), "+v"(accz[1]), "+v"(gate));
  if (aw == 0) STAMP(12);
  {
    const int off = WS_OX + (h * 8 + aw * 2) * 640;
    u32x4 v0, v1, v2;
    _Pragma("nounroll") for (unsigned n = 0; n < SPIN_CAP * 4; ++n) {
      asm volatile("" ::: "memory");
      v0 = GLD(off + (lane >> 5) * 640 + (lane & 31) * 16); v1 = GLD(off + 512); v2 = GLD(off + 640 + 512);
      if (__all(gr_ok(v0, ta, tb) && gr_ok(v1, ta, tb) && gr_ok(v2, ta, tb))) break;
    }
    const float m0 = __uint_as_float(v1[0] << 10), m1 = __uint_as_float(v2[0] << 10);
    const float MM = fmaxf(m0, m1), e0 = __builtin_amdgcn_exp2f((m0 - MM) * LOG2E), e1 = __builtin_amdgcn_exp2f((m1 - MM) * LOG2E);
    const float inv = __builtin_amdgcn_rcpf(fmaf(e0, __uint_as_float(v1[2] << 10), e1 * __uint_as_float(v2[2] << 10)));
    f32x4 o = gr_val(v0) * ((lane < 32 ? e0 : e1) * inv);
#pragma unroll
    for (int r = 0; r < 4; ++r) o[r] = half_sum(o[r]);
    if (lane < 32) *(LAS u32x2*)(lds + L_O + aw * 272 + lane * 8) = u32x2{pk_bf16(o[0], o[1]), pk_bf16(o[2], o[3])};
    WAIT_LGKM0();
    if (lane == 0) __hip_atomic_fetch_add(ctl + 2, 1u, RLX_WG);
  }
  lds_wait_ge<0>(ctl + 2, 8);
  if (aw == 0) STAMP(13);
  {
    f32x4 d0 = {0.f, 0.f, 0.f, 0.f}, d1 = {0.f, 0.f, 0.f, 0.f};
#pragma unroll
    for (int ks = 0; ks < 4; ++ks) {
      const bf16x8 af = *(LAS bf16x8*)(lds + L_O + (lane & 3) * 272 + ks * 64 + g4 * 16);
      d0 = mfma(af, bo[0][ks], d0); d1 = mfma(af, bo[1][ks], d1);
    }
    const int off = WS_YX + (h * 5 + cc) * 2048 + aw * 512 + lane * 16;
    if (lane < 16) { GST(gate * d0 + accz[0], off); GST(gate * d1 + accz[1], off + 256); }
  }
  if (aw == 0) STAMP(14);
  if (h >= 8) return;
  {
    for (int i = 3 - aw; i > 0; --i) __builtin_amdgcn_s_sleep(RSTAG);
    const int off = WS_YX + (g4 * 5 + cc) * 2048 + h * 256 + c16 * 16;
    u32x4 v[8];
    _Pragma("nounroll") for (unsigned n = 0; n < SPIN_CAP * 4; ++n) {
      bool ok = true;
      asm volatile("" ::: "memory");
#pragma unroll
      for (int i = 0; i < 8; ++i) v[i] = GLD(off + i * 20 * 2048);
#pragma unroll
      for (int i = 0; i < 8; ++i) ok &= gr_ok(v[i], ta, tb);
      if (__all(ok)) break;
    }
    f32x4 s = gr_val(v[0]);
#pragma unroll
    for (int i = 1; i < 8; ++i) s += gr_val(v[i]);
    const float s0 = xrow_sum(s[0]), s1 = xrow_sum(s[1]), s2 = xrow_sum(s[2]), s3 = xrow_sum(s[3]);
    const float r = g4 == 0 ? s0 : g4 == 1 ? s1 : g4 == 2 ? s2 : s3;
    p.out[g4 * DM + cc * 128 + h * 16 + c16] = (u16)(pk_bf16(r, 0.f) & 0xffffu);
    if (gid == 0 && aw == 3 && lane == 0) __hip_atomic_store((gu64*)(p.ws + WS_E), E, RLX_AGENT);
    if (aw == 3) STAMP(27);
  }
}

extern "C" void kernel_launch(void* const* d_in, const int* in_sizes, int n_in, void* d_out, int out_size, void* d_ws, size_t ws_size,
                              hipStream_t stream) {
  Params p;
  p.x = (const u16*)d_in[0]; p.k = (const u16*)d_in[1]; p.v = (const u16*)d_in[2]; p.wq = (const u16*)d_in[3]; p.wo = (const u16*)d_in[4];
  p.wg = (const u16*)d_in[6]; p.wu = (const u16*)d_in[7]; p.wd = (const u16*)d_in[8]; p.gw = (const u16*)d_in[9];
  p.out = (u16*)d_out; p.ws = (unsigned char*)d_ws;
  hipLaunchKernelGGL(fused_kernel, dim3(NWG), dim3(512), 0, stream, p);
}
```

```cpp
#include <hip/hip_runtime.h>
#include <hip/hip_bf16.h>
#include <cstdint>

#define GAS __attribute__((address_space(1)))
#define LAS __attribute__((address_space(3)))
typedef unsigned long long u64;
typedef unsigned short u16;
using u32x4 = __attribute__((ext_vector_type(4))) unsigned;
using u32x2 = __attribute__((ext_vector_type(2))) unsigned;
using f32x4 = __attribute__((ext_vector_type(4))) float;
using f32x2 = __attribute__((ext_vector_type(2))) float;
using bf16x2 = __attribute__((ext_vector_type(2))) __bf16;
using bf16x8 = __attribute__((ext_vector_type(8))) __bf16;
using s16x4 = __attribute__((ext_vector_type(4))) short;
typedef GAS u64 gu64;
#define RLX_AGENT __ATOMIC_RELAXED, __HIP_MEMORY_SCOPE_AGENT
#define RLX_WG __ATOMIC_RELAXED, __HIP_MEMORY_SCOPE_WORKGROUP

#define SCHED 37.75
#define THR 12
#define APACE 24
#define QDELAY 60
#define RSTAG 6
#define STAMPS 0

constexpr int DM = 640, NH = 32, SK = 4096, DH = 128, FF = 1728;
constexpr int NWG = 256;
constexpr int WS_E = 0;
constexpr int WS_QX = 4096;
constexpr int WS_GX = WS_QX + 256 * 2048;
constexpr int WS_OX = WS_GX + 96 * 2048;
constexpr int WS_YX = WS_OX + 256 * 640;
constexpr int WS_END = WS_YX + 160 * 2048;
constexpr int WS_ST = WS_END;
constexpr int L_CTL = 0;
constexpr int L_Q = 64;
constexpr int L_H = L_Q + 256;
constexpr int L_O = L_H + 4 * 144;
constexpr int L_MRG = L_O + 4 * 272;
constexpr int L_RED = L_MRG + 4 * 132 * 4;
constexpr int L_XT = L_RED + 16384;
constexpr int L_KV = L_XT + 4 * 448;
constexpr int L_WO = L_KV;
constexpr int L_WD = L_WO + 128 * 272;
constexpr int L_END = L_KV + 4 * 32768;
static_assert(L_O % 16 == 0 && L_H % 16 == 0 && L_MRG % 16 == 0 && L_RED % 16 == 0 && L_KV % 16 == 0 && L_END <= 163840, "lds");

struct Params { const u16* x; const u16* k; const u16* v; const u16* wq; const u16* wo; const u16* wg; const u16* wu; const u16* wd; const u16* gw; u16* out; unsigned char* ws; };

__device__ __forceinline__ float dot2(unsigned a, unsigned b, float c) {
  return __builtin_amdgcn_fdot2_f32_bf16(__builtin_bit_cast(bf16x2, a), __builtin_bit_cast(bf16x2, b), c, false);
}
template <int CTRL> __device__ __forceinline__ float dpp(float x) {
  return __builtin_bit_cast(float, __builtin_amdgcn_mov_dpp(__builtin_bit_cast(int, x), CTRL, 0xf, 0xf, true));
}
__device__ __forceinline__ float row_sum(float x) {
  x += dpp<0x128>(x); x += dpp<0x124>(x); x += dpp<0x122>(x); x += dpp<0x121>(x); return x;
}
__device__ __forceinline__ float half_sum(float x) {
  auto t = __builtin_amdgcn_permlane32_swap(__float_as_uint(x), __float_as_uint(x), false, false);
  return __uint_as_float(t[0]) + __uint_as_float(t[1]);
}
__device__ __forceinline__ float xrow_max(float x) {
  auto s = __builtin_amdgcn_permlane16_swap(__float_as_uint(x), __float_as_uint(x), false, false);
  x = fmaxf(__uint_as_float(s[0]), __uint_as_float(s[1]));
  auto t = __builtin_amdgcn_permlane32_swap(__float_as_uint(x), __float_as_uint(x), false, false);
  return fmaxf(__uint_as_float(t[0]), __uint_as_float(t[1]));
}
__device__ __forceinline__ float xrow_sum(float x) {
  auto s = __builtin_amdgcn_permlane16_swap(__float_as_uint(x), __float_as_uint(x), false, false);
  return half_sum(__uint_as_float(s[0]) + __uint_as_float(s[1]));
}
__device__ __forceinline__ unsigned pk_bf16(float lo, float hi) {
  bf16x2 r = {(__bf16)lo, (__bf16)hi}; return __builtin_bit_cast(unsigned, r);
}
__device__ __forceinline__ float bf_lo(unsigned p) { return __uint_as_float(p << 16); }
__device__ __forceinline__ float bf_hi(unsigned p) { return __uint_as_float(p & 0xffff0000u); }
__device__ __forceinline__ float rbf(float x) { return bf_lo(pk_bf16(x, 0.f) & 0xffffu); }
__device__ __forceinline__ u32x4 ldnt(const u16* p) { return __builtin_nontemporal_load(reinterpret_cast<const u32x4*>(p)); }
__device__ __forceinline__ unsigned ldnt32(const u16* p) { return __builtin_nontemporal_load(reinterpret_cast<const unsigned*>(p)); }
__device__ __forceinline__ void dma16(const u16* gsrc, unsigned lds_dst) {
  unsigned keep;
  asm volatile("s_mov_b32 %0, m0\n\ts_mov_b32 m0, %2\n\ts_nop 0\n\tglobal_load_lds_dwordx4 %1, off nt\n\ts_mov_b32 m0, %0" : "=&s"(keep) : "v"(gsrc), "s"(lds_dst) : "memory");
}
__device__ __forceinline__ bf16x8 tr_frag(LAS unsigned char* a0, LAS unsigned char* a1) {
  s16x4 lo = __builtin_amdgcn_ds_read_tr16_b64_v4i16((LAS s16x4*)a0), hi = __builtin_amdgcn_ds_read_tr16_b64_v4i16((LAS s16x4*)a1);
  return __builtin_bit_cast(bf16x8, __builtin_shufflevector(lo, hi, 0, 1, 2, 3, 4, 5, 6, 7));
}
__device__ __forceinline__ f32x4 mfma(bf16x8 a, bf16x8 b, f32x4 c) { return __builtin_amdgcn_mfma_f32_16x16x32_bf16(a, b, c, 0, 0, 0); }
__device__ __forceinline__ unsigned lds_ld(LAS unsigned* p) { return __hip_atomic_load(p, RLX_WG); }
__device__ __forceinline__ void lds_st(LAS unsigned* p, unsigned v) { __hip_atomic_store(p, v, RLX_WG); }
template <int SLEEP> __device__ __forceinline__ void lds_wait_ge(LAS unsigned* p, unsigned want) {
  _Pragma("nounroll") for (unsigned n = 0; n < (1u << 21); ++n) { if ((unsigned)__builtin_amdgcn_readfirstlane(lds_ld(p)) >= want) break; if (SLEEP) __builtin_amdgcn_s_sleep(SLEEP); }
  asm volatile("" ::: "memory");
}
__device__ __forceinline__ u32x4 gr_pack(f32x4 v, unsigned ta, unsigned tb) {
  const unsigned a = (__float_as_uint(v[0]) + 0x200u) >> 10, b = (__float_as_uint(v[1]) + 0x200u) >> 10, c = (__float_as_uint(v[2]) + 0x200u) >> 10, d = (__float_as_uint(v[3]) + 0x200u) >> 10;
  return u32x4{a | (b << 22), (b >> 10) | ta, c | (d << 22), (d >> 10) | tb};
}
__device__ __forceinline__ bool gr_ok(u32x4 g, unsigned ta, unsigned tb) { return ((g[1] ^ ta) | (g[3] ^ tb)) < 4096u; }
__device__ __forceinline__ f32x4 gr_val(u32x4 g) {
  return f32x4{__uint_as_float(g[0] << 10), __uint_as_float(((g[0] >> 22) << 10) | (g[1] << 20)), __uint_as_float(g[2] << 10), __uint_as_float(((g[2] >> 22) << 10) | (g[3] << 20))};
}
constexpr unsigned SPIN_CAP = 1u << 17;
#define WAIT_LGKM0() asm volatile("s_waitcnt lgkmcnt(0)" ::: "memory")
#define SB() __builtin_amdgcn_sched_barrier(0)
#define VMCNT(n) __builtin_amdgcn_s_waitcnt(0x0F70 | ((n) & 15) | (((n) >> 4) << 14))
#define NOW16() ((unsigned)__builtin_amdgcn_s_memrealtime() * 16u)
#define PACE() do { due += tau; _Pragma("nounroll") while ((int)(NOW16() - due) < 0) __builtin_amdgcn_s_sleep(1); } while (0)
#define GST(v, off) __builtin_amdgcn_raw_buffer_store_b128(gr_pack((v), ta, tb), rs, (off), 0, 16)
#define GLD(off) __builtin_amdgcn_raw_buffer_load_b128(rs, (off), 0, 16)
#define STAMP(i) do {} while (0)

constexpr float LOG2E = 1.4426950408889634f;
constexpr float SCALE = 0.08838834764831845f;

__global__ void __launch_bounds__(512, 2) fused_kernel(Params p) {
  __shared__ __attribute__((aligned(16))) unsigned char smem[L_END];
  LAS unsigned char* lds = (LAS unsigned char*)smem;
  LAS unsigned* ctl = (LAS unsigned*)lds;
  const int lane = threadIdx.x & 63, wave = __builtin_amdgcn_readfirstlane(threadIdx.x >> 6);
  const int bid = blockIdx.x;
  const int b = bid >> 6, h = (bid >> 1) & 31, sp = bid & 1, mem = b * 2 + sp, gid = h * 8 + mem;
  const __amdgpu_buffer_rsrc_t rs = __builtin_amdgcn_make_buffer_rsrc(p.ws, 0, WS_END, 0x00020000);

  if (__builtin_expect(wave < 4, 1)) {
    unsigned tau = (unsigned)(SCHED * 800 / 32);
    unsigned due = NOW16() - tau;
    const size_t eoff = ((size_t)(b * NH + h) * SK + (sp * 4 + wave) * 16) * DH + lane * 8;
    const u16* kp = p.k + eoff; const u16* vp = p.v + eoff;
    constexpr int TSTR = 128 * DH;
    u32x4 kb[4][4], vb[4][4];
    if (wave == 0) STAMP(0);
#pragma unroll
    for (int r = 0; r < 4; ++r) {
      PACE();
#pragma unroll
      for (int t = 0; t < 4; ++t) { VMCNT(THR - 1); kb[r][t] = ldnt(kp + r * TSTR + t * 512); }
      if (r == 0) __builtin_amdgcn_s_barrier();
      PACE();
#pragma unroll
      for (int t = 0; t < 4; ++t) { VMCNT(THR - 1); vb[r][t] = ldnt(vp + r * TSTR + t * 512); }
    }
    const unsigned lkv = (unsigned)(size_t)(lds + L_KV) + wave * 32768;
#define PARK(slot, eo) do { \
      PACE(); \
      _Pragma("unroll") for (int t = 0; t < 4; ++t) { VMCNT(THR - 1); dma16(kp + (eo) + t * 512, lkv + (slot) * 8192 + t * 1024); } \
      PACE(); \
      _Pragma("unroll") for (int t = 0; t < 4; ++t) { VMCNT(THR - 1); dma16(vp + (eo) + t * 512, lkv + (slot) * 8192 + 4096 + t * 1024); } \
    } while (0)
#pragma unroll 1
    for (int r = 0; r < 3; ++r) PARK(r, (4 + r) * TSTR);
    lds_wait_ge<1>(ctl + 15, 1);
    const int nx = (int)__builtin_amdgcn_readfirstlane(ctl[15]) - 1;
    tau = nx == 0 ? (unsigned)(SCHED * 25 * 50 / 48) : nx == 1 ? tau : (unsigned)(SCHED * 25 * 50 / 52);
    const int xo = 31 * TSTR - (nx == 2 ? sp * 64 * DH : 0);
    if (nx) PARK(3, xo);
    if (wave == 0) STAMP(1);
    lds_wait_ge<1>(ctl + 0, 1);
    const u32x4 q = *(LAS u32x4*)(lds + L_Q + (lane & 15) * 16);
    float m = -INFINITY, l = 0.f, acc[8];
#pragma unroll
    for (int j = 0; j < 8; ++j) acc[j] = 0.f;
    kp += 7 * TSTR; vp += 7 * TSTR;
#define TILE(KB, VB, r, ISSUE) do { \
      float s[4]; \
      _Pragma("unroll") for (int t = 0; t < 4; ++t) s[t] = dot2(KB[t][3], q[3], dot2(KB[t][2], q[2], dot2(KB[t][1], q[1], dot2(KB[t][0], q[0], 0.f)))); \
      asm volatile("" : "+v"(s[0]), "+v"(s[1]), "+v"(s[2]), "+v"(s[3])); \
      if (ISSUE) { SB(); PACE(); _Pragma("unroll") for (int t = 0; t < 4; ++t) { VMCNT(THR - 1); KB[t] = ldnt(kp + r * TSTR + t * 512); } SB(); } \
      _Pragma("unroll") for (int t = 0; t < 4; ++t) s[t] = row_sum(s[t]); \
      { unsigned a = pk_bf16(s[0], s[1]), c = pk_bf16(s[2], s[3]); \
        a = pk_bf16(bf_lo(a) * SCALE, bf_hi(a) * SCALE); c = pk_bf16(bf_lo(c) * SCALE, bf_hi(c) * SCALE); \
        s[0] = bf_lo(a); s[1] = bf_hi(a); s[2] = bf_lo(c); s[3] = bf_hi(c); } \
      const float mn = fmaxf(fmaxf(fmaxf(s[0], s[1]), fmaxf(s[2], s[3])), m); \
      const float al = __builtin_amdgcn_exp2f((m - mn) * LOG2E); m = mn; \
      _Pragma("unroll") for (int t = 0; t < 4; ++t) s[t] = __builtin_amdgcn_exp2f((s[t] - mn) * LOG2E); \
      l = fmaf(l, al, (s[0] + s[1]) + (s[2] + s[3])); \
      _Pragma("unroll") for (int j = 0; j < 8; ++j) acc[j] *= al; \
      _Pragma("unroll") for (int t = 0; t < 4; ++t) { \
        _Pragma("unroll") for (int j = 0; j < 4; ++j) { acc[2 * j] = fmaf(s[t], bf_lo(VB[t][j]), acc[2 * j]); acc[2 * j + 1] = fmaf(s[t], bf_hi(VB[t][j]), acc[2 * j + 1]); } } \
      asm volatile("" : "+v"(acc[0]), "+v"(acc[1]), "+v"(acc[2]), "+v"(acc[3]), "+v"(acc[4]), "+v"(acc[5]), "+v"(acc[6]), "+v"(acc[7]), "+v"(l)); \
      SB(); if (ISSUE) { PACE(); _Pragma("unroll") for (int t = 0; t < 4; ++t) { VMCNT(THR - 1); VB[t] = ldnt(vp + r * TSTR + t * 512); } SB(); } \
    } while (0)
#pragma unroll 1
    for (int it = 0; it < 6; ++it) {
      TILE(kb[0], vb[0], 0, true); TILE(kb[1], vb[1], 1, true); TILE(kb[2], vb[2], 2, true); TILE(kb[3], vb[3], 3, true);
      kp += 4 * TSTR; vp += 4 * TSTR;
      if (it < 3 + nx) {
        u32x4 kl[4], vl[4];
        LAS unsigned char* src = lds + L_KV + wave * 32768 + (it & 3) * 8192 + lane * 16;
#pragma unroll
        for (int t = 0; t < 4; ++t) { kl[t] = *(LAS u32x4*)(src + t * 1024); vl[t] = *(LAS u32x4*)(src + 4096 + t * 1024); }
        TILE(kl, vl, 0, false);
        if (it == 2 + nx && lane == 0) __hip_atomic_fetch_add(ctl + 13, 1u, RLX_WG);
        if (it == 1 && nx == 2) PARK(0, xo - 15 * TSTR + 64 * DH);
      }
    }
    if (lane == 0) lds_st(ctl + 14, 1u);
    TILE(kb[0], vb[0], 0, false); TILE(kb[1], vb[1], 1, false); TILE(kb[2], vb[2], 2, false); TILE(kb[3], vb[3], 3, false);
#undef TILE
    STAMP(16 + wave);
    const float M = xrow_max(m), f = __builtin_amdgcn_exp2f((m - M) * LOG2E);
    l = xrow_sum(l * f);
#pragma unroll
    for (int j = 0; j < 8; ++j) acc[j] = xrow_sum(acc[j] * f);
    LAS float* rec = (LAS float*)(lds + L_MRG) + wave * 132;
    if (lane < 16) {
      *(LAS f32x4*)(rec + 4 + lane * 8) = f32x4{acc[0], acc[1], acc[2], acc[3]};
      *(LAS f32x4*)(rec + 8 + lane * 8) = f32x4{acc[4], acc[5], acc[6], acc[7]};
      if (lane == 0) *(LAS f32x2*)rec = f32x2{M, l};
    }
    WAIT_LGKM0();
    unsigned old = 0;
    if (lane == 0) old = __hip_atomic_fetch_add(ctl + 1, 1u, RLX_WG);
    old = __builtin_amdgcn_readfirstlane(old);
    if (old != 3) return;
    asm volatile("" ::: "memory");
    LAS float* mr = (LAS float*)(lds + L_MRG);
    float mw[4], lw[4]; f32x4 av[4];
#pragma unroll
    for (int w = 0; w < 4; ++w) { f32x2 t = *(LAS f32x2*)(mr + w * 132); mw[w] = t[0]; lw[w] = t[1]; av[w] = *(LAS f32x4*)(mr + w * 132 + 4 + 4 * (lane & 31)); }
    const unsigned ta = ctl[4], tb = ctl[5];
    const float MM = fmaxf(fmaxf(mw[0], mw[1]), fmaxf(mw[2], mw[3]));
    float L = 0.f; f32x4 o = {0.f, 0.f, 0.f, 0.f};
#pragma unroll
    for (int w = 0; w < 4; ++w) { const float g = __builtin_amdgcn_exp2f((mw[w] - MM) * LOG2E); L = fmaf(g, lw[w], L); o += g * av[w]; }
    if (lane == 32) o = f32x4{MM, 0.f, L, 0.f};
    if (lane <= 32) GST(o, WS_OX + gid * 640 + lane * 16);
    STAMP(5);
    return;
  }

  const int aw = wave - 4, g4 = lane >> 4, c16 = lane & 15;
  u64 E = 0;
  unsigned ta = 0, tb = 0;
  unsigned msk[4];
#pragma unroll
  for (int d = 0; d < 4; ++d) msk[d] = ((c16 & 7) >> 1) == d ? ((c16 & 1) ? 0xffff0000u : 0x0000ffffu) : 0u;
  const unsigned sel = (c16 & 8) ? 0x03020302u : 0x01000100u;
  LAS unsigned char* xt = lds + L_XT + aw * 448;
#define PROJECT(NL, W, REDOFF, CNT, DSTOFF, ST) do { \
    f32x4 d0 = {0.f, 0.f, 0.f, 0.f}, d1 = {0.f, 0.f, 0.f, 0.f}; \
    _Pragma("unroll") for (int i = 0; i < NL; ++i) { \
      const u32x2 xx = *(LAS u32x2*)(xt + (i * 4 + g4) * 8); \
      const unsigned x0 = __builtin_amdgcn_perm(xx[0], xx[0], sel), x1 = __builtin_amdgcn_perm(xx[1], xx[1], sel); \
      d0 = mfma(__builtin_bit_cast(bf16x8, W[i]), __builtin_bit_cast(bf16x8, u32x4{x0 & msk[0], x0 & msk[1], x0 & msk[2], x0 & msk[3]}), d0); \
      d1 = mfma(__builtin_bit_cast(bf16x8, W[i]), __builtin_bit_cast(bf16x8, u32x4{x1 & msk[0], x1 & msk[1], x1 & msk[2], x1 & msk[3]}), d1); \
    } \
    LAS f32x4* red = (LAS f32x4*)(lds + L_RED + REDOFF); \
    red[(aw * 2 + 0) * 64 + lane] = d0; red[(aw * 2 + 1) * 64 + lane] = d1; \
    WAIT_LGKM0(); \
    unsigned old = 0; \
    if (lane == 0) old = __hip_atomic_fetch_add(ctl + CNT, 1u, RLX_WG); \
    old = __builtin_amdgcn_readfirstlane(old); \
    lds_wait_ge<0>(ctl + 6, 1); ta = ctl[4]; tb = ctl[5]; \
    if (old == 3) {     \
      d0 = (red[lane] + red[128 + lane]) + (red[256 + lane] + red[384 + lane]); \
      d1 = (red[64 + lane] + red[192 + lane]) + (red[320 + lane] + red[448 + lane]); \
      const int off = (DSTOFF) + (c16 >> 3) * 512 + ((c16 & 7) * 4 + g4) * 16; \
      GST(d0, off); GST(d1, off + 1024); \
      STAMP(ST); \
    } } while (0)
#define XTABLE(NROWS2) do { \
    u32x4 t = {__builtin_amdgcn_perm(xr[1], xr[0], 0x05040100u), __builtin_amdgcn_perm(xr[3], xr[2], 0x05040100u), \
               __builtin_amdgcn_perm(xr[1], xr[0], 0x07060302u), __builtin_amdgcn_perm(xr[3], xr[2], 0x07060302u)}; \
    if (lane >= (NROWS2)) t = u32x4{0u, 0u, 0u, 0u}; \
    if (lane < 28) *(LAS u32x4*)(xt + lane * 16) = t; \
    WAIT_LGKM0(); } while (0)
  {
    const int r0 = mem * 80 + aw * 20;
    u32x4 w[5]; unsigned xr[4];
#pragma unroll
    for (int i = 0; i < 5; ++i) w[i] = ldnt(p.wq + (size_t)(r0 + i * 4 + g4) * (NH * DH) + h * DH + c16 * 8);
#pragma unroll
    for (int bb = 0; bb < 4; ++bb) xr[bb] = ldnt32(p.x + bb * DM + r0 + 2 * min(lane, 9));
    if (aw == 3) E = __hip_atomic_load((gu64*)(p.ws + WS_E), RLX_AGENT);
    if (aw == 0 && lane < 16) lds_st(ctl + lane, 0u);
    WAIT_LGKM0();
    __builtin_amdgcn_s_barrier();
    XTABLE(10);
    asm volatile("" : "+v"(E));
    E += 1;
    if (aw == 3 && lane == 0) { ctl[4] = (unsigned)E << 12; ctl[5] = ((unsigned)(E >> 20) + (unsigned)E * 0x9E5u) << 12; WAIT_LGKM0(); lds_st(ctl + 6, 1u); }
    const unsigned odd = __builtin_amdgcn_s_getreg(20 | (3 << 11)) & 1u;
    if (aw == 3) { ta = ctl[4]; tb = ctl[5]; if (lane == 0) GST((f32x4{odd ? 1.f : 0.f, 0.f, 0.f, 0.f}), WS_OX + gid * 640 + 33 * 16); }
    PROJECT(5, w, 0, 7, WS_QX + gid * 2048, 8);
    if (aw == 2) {
      u32x4 v;
      _Pragma("nounroll") for (unsigned n = 0; n < SPIN_CAP; ++n) {
        asm volatile("" ::: "memory");
        v = GLD(WS_OX + (gid ^ 1) * 640 + 33 * 16);
        if (__all(gr_ok(v, ta, tb))) break;
        __builtin_amdgcn_s_sleep(8);
      }
      const unsigned po = (v[0] << 10) != 0u;
      if (lane == 0) lds_st(ctl + 15, po == odd ? 2u : odd ? 1u : 3u);
      STAMP(7);
    }
    if (aw == 3) {
      const int off = WS_QX + h * 16384 + (lane >> 5) * 2048 + b * 512 + (lane & 31) * 16;
      u32x4 v[4];
      __builtin_amdgcn_s_sleep(QDELAY);
      _Pragma("nounroll") for (unsigned n = 0; n < SPIN_CAP; ++n) {
        asm volatile("" ::: "memory");
#pragma unroll
        for (int i = 0; i < 4; ++i) v[i] = GLD(off + i * 4096);
        if (__all(gr_ok(v[0], ta, tb) && gr_ok(v[1], ta, tb) && gr_ok(v[2], ta, tb) && gr_ok(v[3], ta, tb))) break;
      }
      const f32x4 a = (gr_val(v[0]) + gr_val(v[1])) + (gr_val(v[2]) + gr_val(v[3]));
#pragma unroll
      for (int r = 0; r < 4; ++r) {
        const float qv = half_sum(a[r]);
        if (lane < 32) *(LAS u16*)(lds + L_Q + ((lane & 3) * 32 + r * 8 + (lane >> 2)) * 2) = (u16)pk_bf16(qv, 0.f);
      }
      WAIT_LGKM0();
      if (lane == 0) lds_st(ctl + 0, 1u);
      STAMP(11);
    }
    lds_wait_ge<4>(ctl + 0, 1);
  }
  if ((mem & 1) && mem > 1) {
    if (h >= 27) return;
    const int part = (mem - 3) >> 1, nl = part == 0 ? 14 : 13;
    const int r0 = (part == 0 ? 0 : 16 + part * 208) + aw * nl * 4;
    u32x4 w[14]; unsigned xr[4];
#pragma unroll
    for (int bb = 0; bb < 4; ++bb) xr[bb] = ldnt32(p.x + bb * DM + r0 + 2 * min(lane, nl * 2 - 1));
    const u16* src = (c16 < 8 ? p.wg : p.wu) + h * 64 + (c16 & 7) * 8;
#pragma unroll
    for (int i = 0; i < 14; ++i) { __builtin_amdgcn_s_sleep(APACE); VMCNT(3); w[i] = ldnt(src + (size_t)(r0 + min(i, nl - 1) * 4 + g4) * FF); }
    XTABLE(nl * 2);
    PROJECT(14, w, 8192, 12, WS_GX + (h * 3 + part) * 2048, 9);
    return;
  }

  const int cc = mem < 2 ? mem : (mem >> 1) + 1;
  const unsigned trq = (lane & 15) >> 2, trp = lane & 3;
  if (aw == 1) {
    unsigned gwv[5], xg[4][5];
#pragma unroll
    for (int i = 0; i < 5; ++i) {
      gwv[i] = ldnt32(p.gw + i * 128 + lane * 2);
#pragma unroll
      for (int bb = 0; bb < 4; ++bb) xg[bb][i] = ldnt32(p.x + bb * DM + i * 128 + lane * 2);
    }
#pragma unroll
    for (int bb = 0; bb < 4; ++bb) {
      float s = 0.f;
#pragma unroll
      for (int i = 0; i < 5; ++i) { const unsigned pr = pk_bf16(bf_lo(xg[bb][i]) * bf_lo(gwv[i]), bf_hi(xg[bb][i]) * bf_hi(gwv[i])); s += bf_lo(pr) + bf_hi(pr); }
      s = rbf(xrow_sum(row_sum(s)));
      const float gt = rbf(1.f / (1.f + __expf(-s)));
      if (lane == 0) *(LAS float*)(lds + L_CTL + 32 + bb * 4) = gt;
    }
  } else if (aw == 3 && h < 27) {
    const int off = WS_GX + h * 6144 + lane * 16;
    u32x4 v[6];
    _Pragma("nounroll") for (unsigned n = 0; n < SPIN_CAP; ++n) {
      bool ok = true;
      __builtin_amdgcn_s_sleep(64);
      asm volatile("" ::: "memory");
#pragma unroll
      for (int i = 0; i < 6; ++i) v[i] = GLD(off + i * 1024);
#pragma unroll
      for (int i = 0; i < 6; ++i) ok &= gr_ok(v[i], ta, tb);
      if (__all(ok)) break;
    }
#pragma unroll
    for (int u = 0; u < 2; ++u) {
      const f32x4 s = (gr_val(v[u]) + gr_val(v[2 + u])) + gr_val(v[4 + u]);
#pragma unroll
      for (int r = 0; r < 4; ++r) {
        const float a = rbf(s[r]), hv = a * dpp<0x4E>(a);
        if ((lane & 2) == 0) *(LAS u16*)(lds + L_H + (u * 2 + (lane >> 5)) * 144 + (((lane & 1) * 4 + r) * 8 + ((lane >> 2) & 7)) * 2) = (u16)pk_bf16(hv, 0.f);
      }
    }
    STAMP(10);
  }
  {
    lds_wait_ge<4>(ctl + 14, 1);
    u32x4 wv[8], wd[4];
#pragma unroll
    for (int i = 0; i < 8; ++i) wv[i] = ldnt(p.wo + (size_t)(h * DH + aw * 32 + i * 4 + g4) * DM + cc * 128 + c16 * 8);
    if (h < 27) {
#pragma unroll
      for (int i = 0; i < 4; ++i) wd[i] = ldnt(p.wd + (size_t)(h * 64 + aw * 16 + i * 4 + g4) * DM + cc * 128 + c16 * 8);
    }
    lds_wait_ge<1>(ctl + 13, 4);
#pragma unroll
    for (int i = 0; i < 8; ++i) *(LAS u32x4*)(lds + L_WO + (aw * 32 + i * 4 + g4) * 272 + c16 * 16) = wv[i];
    if (h < 27) {
#pragma unroll
      for (int i = 0; i < 4; ++i) *(LAS u32x4*)(lds + L_WD + (aw * 16 + i * 4 + g4) * 272 + c16 * 16) = wd[i];
    }
  }
  WAIT_LGKM0();
  if (lane == 0) __hip_atomic_fetch_add(ctl + 2, 1u, RLX_WG);
  lds_wait_ge<0>(ctl + 2, 4);
  bf16x8 bo[2][4];
  f32x4 accz[2] = {f32x4{0.f, 0.f, 0.f, 0.f}, f32x4{0.f, 0.f, 0.f, 0.f}};
  {
    LAS unsigned char* tw = lds + L_WO + (g4 * 8 + trq) * 272 + trp * 8 + aw * 64;
#pragma unroll
    for (int ks = 0; ks < 4; ++ks) { bo[0][ks] = tr_frag(tw + ks * 8704, tw + ks * 8704 + 1088); bo[1][ks] = tr_frag(tw + ks * 8704 + 32, tw + ks * 8704 + 1088 + 32); }
    if (h < 27) {
      LAS unsigned char* td = lds + L_WD + (g4 * 8 + trq) * 272 + trp * 8 + aw * 64;
#pragma unroll
      for (int ks = 0; ks < 2; ++ks) {
        const bf16x8 af = *(LAS bf16x8*)(lds + L_H + (lane & 3) * 144 + ks * 64 + g4 * 16);
        accz[0] = mfma(af, tr_frag(td + ks * 8704, td + ks * 8704 + 1088), accz[0]);
        accz[1] = mfma(af, tr_frag(td + ks * 8704 + 32, td + ks * 8704 + 1088 + 32), accz[1]);
      }
    }
  }
  f32x4 gate = *(LAS f32x4*)(lds + L_CTL + 32);
#pragma unroll
  for (int i = 0; i < 4; ++i) { const float og = rbf(1.f - gate[i]); accz[0][i] *= og; accz[1][i] *= og; }
  asm volatile("" : "+v"(accz[0]), "+v"(accz[1]), "+v"(gate));
  if (aw == 0) STAMP(12);
  {
    const int off = WS_OX + (h * 8 + aw * 2) * 640;
    u32x4 v0, v1, v2;
    _Pragma("nounroll") for (unsigned n = 0; n < SPIN_CAP * 4; ++n) {
      asm volatile("" ::: "memory");
      v0 = GLD(off + (lane >> 5) * 640 + (lane & 31) * 16); v1 = GLD(off + 512); v2 = GLD(off + 640 + 512);
      if (__all(gr_ok(v0, ta, tb) && gr_ok(v1, ta, tb) && gr_ok(v2, ta, tb))) break;
    }
    const float m0 = __uint_as_float(v1[0] << 10), m1 = __uint_as_float(v2[0] << 10);
    const float MM = fmaxf(m0, m1), e0 = __builtin_amdgcn_exp2f((m0 - MM) * LOG2E), e1 = __builtin_amdgcn_exp2f((m1 - MM) * LOG2E);
    const float inv = __builtin_amdgcn_rcpf(fmaf(e0, __uint_as_float(v1[2] << 10), e1 * __uint_as_float(v2[2] << 10)));
    f32x4 o = gr_val(v0) * ((lane < 32 ? e0 : e1) * inv);
#pragma unroll
    for (int r = 0; r < 4; ++r) o[r] = half_sum(o[r]);
    if (lane < 32) *(LAS u32x2*)(lds + L_O + aw * 272 + lane * 8) = u32x2{pk_bf16(o[0], o[1]), pk_bf16(o[2], o[3])};
    WAIT_LGKM0();
    if (lane == 0) __hip_atomic_fetch_add(ctl + 2, 1u, RLX_WG);
  }
  lds_wait_ge<0>(ctl + 2, 8);
  if (aw == 0) STAMP(13);
  {
    f32x4 d0 = {0.f, 0.f, 0.f, 0.f}, d1 = {0.f, 0.f, 0.f, 0.f};
#pragma unroll
    for (int ks = 0; ks < 4; ++ks) {
      const bf16x8 af = *(LAS bf16x8*)(lds + L_O + (lane & 3) * 272 + ks * 64 + g4 * 16);
      d0 = mfma(af, bo[0][ks], d0); d1 = mfma(af, bo[1][ks], d1);
    }
    const int off = WS_YX + (h * 5 + cc) * 2048 + aw * 512 + lane * 16;
    if (lane < 16) { GST(gate * d0 + accz[0], off); GST(gate * d1 + accz[1], off + 256); }
  }
  if (aw == 0) STAMP(14);
  if (h >= 8) return;
  {
    for (int i = 3 - aw; i > 0; --i) __builtin_amdgcn_s_sleep(RSTAG);
    const int off = WS_YX + (g4 * 5 + cc) * 2048 + h * 256 + c16 * 16;
    u32x4 v[8];
    _Pragma("nounroll") for (unsigned n = 0; n < SPIN_CAP * 4; ++n) {
      bool ok = true;
      asm volatile("" ::: "memory");
#pragma unroll
      for (int i = 0; i < 8; ++i) v[i] = GLD(off + i * 20 * 2048);
#pragma unroll
      for (int i = 0; i < 8; ++i) ok &= gr_ok(v[i], ta, tb);
      if (__all(ok)) break;
    }
    f32x4 s = gr_val(v[0]);
#pragma unroll
    for (int i = 1; i < 8; ++i) s += gr_val(v[i]);
    const float s0 = xrow_sum(s[0]), s1 = xrow_sum(s[1]), s2 = xrow_sum(s[2]), s3 = xrow_sum(s[3]);
    const float r = g4 == 0 ? s0 : g4 == 1 ? s1 : g4 == 2 ? s2 : s3;
    p.out[g4 * DM + cc * 128 + h * 16 + c16] = (u16)(pk_bf16(r, 0.f) & 0xffffu);
    if (gid == 0 && aw == 3 && lane == 0) __hip_atomic_store((gu64*)(p.ws + WS_E), E, RLX_AGENT);
    if (aw == 3) STAMP(27);
  }
}

extern "C" void kernel_launch(void* const* d_in, const int* in_sizes, int n_in, void* d_out, int out_size, void* d_ws, size_t ws_size,
                              hipStream_t stream) {
  Params p;
  p.x = (const u16*)d_in[0]; p.k = (const u16*)d_in[1]; p.v = (const u16*)d_in[2]; p.wq = (const u16*)d_in[3]; p.wo = (const u16*)d_in[4];
  p.wg = (const u16*)d_in[6]; p.wu = (const u16*)d_in[7]; p.wd = (const u16*)d_in[8]; p.gw = (const u16*)d_in[9];
  p.out = (u16*)d_out; p.ws = (unsigned char*)d_ws;
  hipLaunchKernelGGL(fused_kernel, dim3(NWG), dim3(512), 0, stream, p);
}
```

```cpp
#include <hip/hip_runtime.h>
#include <hip/hip_bf16.h>
#include <cstdint>

#define GAS __attribute__((address_space(1)))
#define LAS __attribute__((address_space(3)))
typedef unsigned long long u64;
typedef unsigned short u16;
using u32x4 = __attribute__((ext_vector_type(4))) unsigned;
using u32x2 = __attribute__((ext_vector_type(2))) unsigned;
using f32x4 = __attribute__((ext_vector_type(4))) float;
using f32x2 = __attribute__((ext_vector_type(2))) float;
using bf16x2 = __attribute__((ext_vector_type(2))) __bf16;
using bf16x8 = __attribute__((ext_vector_type(8))) __bf16;
using s16x4 = __attribute__((ext_vector_type(4))) short;
typedef GAS u64 gu64;
#define RLX_AGENT __ATOMIC_RELAXED, __HIP_MEMORY_SCOPE_AGENT
#define RLX_WG __ATOMIC_RELAXED, __HIP_MEMORY_SCOPE_WORKGROUP

#define SCHED 37.6
#define THR 12
#define APACE 24
#define QDELAY 60
#define RSTAG 6
#define REANCHOR 600
#define GWAIT 30

constexpr int DM = 640, NH = 32, SK = 4096, DH = 128, FF = 1728;
constexpr int NWG = 256;
constexpr int WS_E = 0;
constexpr int WS_QX = 4096;
constexpr int WS_GX = WS_QX + 256 * 4096;
constexpr int WS_OX = WS_GX + 96 * 2048;
constexpr int WS_YX = WS_OX + 256 * 640;
constexpr int WS_END = WS_YX + 160 * 2048;
constexpr int L_CTL = 0;
constexpr int L_Q = 64;
constexpr int L_H = L_Q + 256;
constexpr int L_O = L_H + 4 * 144;
constexpr int L_MRG = L_O + 4 * 272;
constexpr int L_RED = L_MRG + 4 * 132 * 4;
constexpr int L_XT = L_RED + 16384;
constexpr int L_KV = L_XT + 4 * 448;
constexpr int L_WO = L_KV;
constexpr int L_WD = L_WO + 128 * 272;
constexpr int L_END = L_KV + 4 * 32768;
static_assert(L_O % 16 == 0 && L_H % 16 == 0 && L_MRG % 16 == 0 && L_RED % 16 == 0 && L_KV % 16 == 0 && L_END <= 163840, "lds");

constexpr unsigned TAU0 = (unsigned)(SCHED * 25);
struct Params { const u16* x; const u16* k; const u16* v; const u16* wq; const u16* wo; const u16* wg; const u16* wu; const u16* wd; const u16* gw; u16* out; unsigned char* ws; };

__device__ __forceinline__ float dot2(unsigned a, unsigned b, float c) {
  return __builtin_amdgcn_fdot2_f32_bf16(__builtin_bit_cast(bf16x2, a), __builtin_bit_cast(bf16x2, b), c, false);
}
template <int CTRL> __device__ __forceinline__ float dpp(float x) {
  return __builtin_bit_cast(float, __builtin_amdgcn_mov_dpp(__builtin_bit_cast(int, x), CTRL, 0xf, 0xf, true));
}
__device__ __forceinline__ float row_sum(float x) {
  x += dpp<0x128>(x); x += dpp<0x124>(x); x += dpp<0x122>(x); x += dpp<0x121>(x); return x;
}
__device__ __forceinline__ float half_sum(float x) {
  auto t = __builtin_amdgcn_permlane32_swap(__float_as_uint(x), __float_as_uint(x), false, false);
  return __uint_as_float(t[0]) + __uint_as_float(t[1]);
}
__device__ __forceinline__ float xrow_max(float x) {
  auto s = __builtin_amdgcn_permlane16_swap(__float_as_uint(x), __float_as_uint(x), false, false);
  x = fmaxf(__uint_as_float(s[0]), __uint_as_float(s[1]));
  auto t = __builtin_amdgcn_permlane32_swap(__float_as_uint(x), __float_as_uint(x), false, false);
  return fmaxf(__uint_as_float(t[0]), __uint_as_float(t[1]));
}
__device__ __forceinline__ float xrow_sum(float x) {
  auto s = __builtin_amdgcn_permlane16_swap(__float_as_uint(x), __float_as_uint(x), false, false);
  return half_sum(__uint_as_float(s[0]) + __uint_as_float(s[1]));
}
__device__ __forceinline__ unsigned pk_bf16(float lo, float hi) {
  bf16x2 r = {(__bf16)lo, (__bf16)hi}; return __builtin_bit_cast(unsigned, r);
}
__device__ __forceinline__ float bf_lo(unsigned p) { return __uint_as_float(p << 16); }
__device__ __forceinline__ float bf_hi(unsigned p) { return __uint_as_float(p & 0xffff0000u); }
__device__ __forceinline__ float rbf(float x) { return bf_lo(pk_bf16(x, 0.f) & 0xffffu); }
__device__ __forceinline__ u32x4 ldnt(const u16* p) { return __builtin_nontemporal_load(reinterpret_cast<const u32x4*>(p)); }
__device__ __forceinline__ unsigned ldnt32(const u16* p) { return __builtin_nontemporal_load(reinterpret_cast<const unsigned*>(p)); }
__device__ __forceinline__ void dma16(const u16* gsrc, unsigned lds_dst) {
  unsigned keep;
  asm volatile("s_mov_b32 %0, m0\n\ts_mov_b32 m0, %2\n\ts_nop 0\n\tglobal_load_lds_dwordx4 %1, off nt\n\ts_mov_b32 m0, %0" : "=&s"(keep) : "v"(gsrc), "s"(lds_dst) : "memory");
}
__device__ __forceinline__ bf16x8 tr_frag(LAS unsigned char* a0, LAS unsigned char* a1) {
  s16x4 lo = __builtin_amdgcn_ds_read_tr16_b64_v4i16((LAS s16x4*)a0), hi = __builtin_amdgcn_ds_read_tr16_b64_v4i16((LAS s16x4*)a1);
  return __builtin_bit_cast(bf16x8, __builtin_shufflevector(lo, hi, 0, 1, 2, 3, 4, 5, 6, 7));
}
__device__ __forceinline__ f32x4 mfma(bf16x8 a, bf16x8 b, f32x4 c) { return __builtin_amdgcn_mfma_f32_16x16x32_bf16(a, b, c, 0, 0, 0); }
__device__ __forceinline__ unsigned lds_ld(LAS unsigned* p) { return __hip_atomic_load(p, RLX_WG); }
__device__ __forceinline__ void lds_st(LAS unsigned* p, unsigned v) { __hip_atomic_store(p, v, RLX_WG); }
template <int SLEEP> __device__ __forceinline__ void lds_wait_ge(LAS unsigned* p, unsigned want) {
  _Pragma("nounroll") for (unsigned n = 0; n < (1u << 21); ++n) { if ((unsigned)__builtin_amdgcn_readfirstlane(lds_ld(p)) >= want) break; if (SLEEP) __builtin_amdgcn_s_sleep(SLEEP); }
  asm volatile("" ::: "memory");
}
__device__ __forceinline__ u32x4 gr_pack(f32x4 v, unsigned ta, unsigned tb) {
  const unsigned a = (__float_as_uint(v[0]) + 0x200u) >> 10, b = (__float_as_uint(v[1]) + 0x200u) >> 10, c = (__float_as_uint(v[2]) + 0x200u) >> 10, d = (__float_as_uint(v[3]) + 0x200u) >> 10;
  return u32x4{a | (b << 22), (b >> 10) | ta, c | (d << 22), (d >> 10) | tb};
}
__device__ __forceinline__ bool gr_ok(u32x4 g, unsigned ta, unsigned tb) { return ((g[1] ^ ta) | (g[3] ^ tb)) < 4096u; }
__device__ __forceinline__ f32x4 gr_val(u32x4 g) {
  return f32x4{__uint_as_float(g[0] << 10), __uint_as_float(((g[0] >> 22) << 10) | (g[1] << 20)), __uint_as_float(g[2] << 10), __uint_as_float(((g[2] >> 22) << 10) | (g[3] << 20))};
}
constexpr unsigned SPIN_CAP = 1u << 17;
#define WAIT_LGKM0() asm volatile("s_waitcnt lgkmcnt(0)" ::: "memory")
#define SB() __builtin_amdgcn_sched_barrier(0)
#define VMCNT(n) __builtin_amdgcn_s_waitcnt(0x0F70 | ((n) & 15) | (((n) >> 4) << 14))
#define NOW16() ((unsigned)__builtin_amdgcn_s_memrealtime() * 16u)
#define PACE() do { due += tau; _Pragma("nounroll") while ((int)(NOW16() - due) < 0) __builtin_amdgcn_s_sleep(1); } while (0)
#define GST(v, off) __builtin_amdgcn_raw_buffer_store_b128(gr_pack((v), ta, tb), rs, (off), 0, 16)
#define GSTW(a, b, off) __builtin_amdgcn_raw_buffer_store_b128(u32x4{__float_as_uint(a), ta | (tb >> 20), __float_as_uint(b), tb | (ta >> 20)}, rs, (off), 0, 16)
#define GLD(off) __builtin_amdgcn_raw_buffer_load_b128(rs, (off), 0, 16)

constexpr float LOG2E = 1.4426950408889634f;
constexpr float SCALE = 0.08838834764831845f;

__global__ void __launch_bounds__(512, 2) fused_kernel(Params p) {
  __shared__ __attribute__((aligned(16))) unsigned char smem[L_END];
  LAS unsigned char* lds = (LAS unsigned char*)smem;
  LAS unsigned* ctl = (LAS unsigned*)lds;
  const int lane = threadIdx.x & 63, wave = __builtin_amdgcn_readfirstlane(threadIdx.x >> 6);
  const int bid = blockIdx.x;
  const int b = bid >> 6, h = (bid >> 1) & 31, sp = bid & 1, mem = b * 2 + sp, gid = h * 8 + mem;
  const __amdgpu_buffer_rsrc_t rs = __builtin_amdgcn_make_buffer_rsrc(p.ws, 0, WS_END, 0x00020000);

  if (__builtin_expect(wave < 4, 1)) {
    unsigned tau = TAU0;
    unsigned due = NOW16() - tau;
    const size_t eoff = ((size_t)(b * NH + h) * SK + (sp * 4 + wave) * 16) * DH + lane * 8;
    const u16* kp = p.k + eoff; const u16* vp = p.v + eoff;
    constexpr int TSTR = 128 * DH;
    u32x4 kb[4][4], vb[4][4];
#pragma unroll
    for (int r = 0; r < 4; ++r) {
      PACE();
#pragma unroll
      for (int t = 0; t < 4; ++t) { VMCNT(THR - 1); kb[r][t] = ldnt(kp + r * TSTR + t * 512); }
      if (r == 0) __builtin_amdgcn_s_barrier();
      PACE();
#pragma unroll
      for (int t = 0; t < 4; ++t) { VMCNT(THR - 1); vb[r][t] = ldnt(vp + r * TSTR + t * 512); }
      if (r == 0) {
        VMCNT(3);
        const unsigned now = NOW16() - tau - REANCHOR;
        if ((int)(now - due) > 0) due = now;
      }
    }
    const unsigned lkv = (unsigned)(size_t)(lds + L_KV) + wave * 32768;
#define PARK(slot, eo) do { \
      PACE(); \
      _Pragma("unroll") for (int t = 0; t < 4; ++t) { VMCNT(THR - 1); dma16(kp + (eo) + t * 512, lkv + (slot) * 8192 + t * 1024); } \
      PACE(); \
      _Pragma("unroll") for (int t = 0; t < 4; ++t) { VMCNT(THR - 1); dma16(vp + (eo) + t * 512, lkv + (slot) * 8192 + 4096 + t * 1024); } \
    } while (0)
#pragma unroll 1
    for (int r = 0; r < 3; ++r) PARK(r, (4 + r) * TSTR);
    lds_wait_ge<1>(ctl + 15, 1);
    const int nx = (int)__builtin_amdgcn_readfirstlane(ctl[15]) - 1;
    tau = nx == 0 ? TAU0 * 50u / 48u : nx == 1 ? tau : TAU0 * 50u / 52u;
    const int xo = 31 * TSTR - (nx == 2 ? sp * 64 * DH : 0);
    if (nx) PARK(3, xo);
    lds_wait_ge<1>(ctl + 0, 1);
    const u32x4 q = *(LAS u32x4*)(lds + L_Q + (lane & 15) * 16);
    float m = -INFINITY, l = 0.f, acc[8];
#pragma unroll
    for (int j = 0; j < 8; ++j) acc[j] = 0.f;
    kp += 7 * TSTR; vp += 7 * TSTR;
#define TILE(KB, VB, r, ISSUE) do { \
      float s[4]; \
      if (ISSUE) { SB(); PACE(); _Pragma("unroll") for (int t = 0; t < 4; ++t) { VMCNT(THR - 1); kb[r][t] = ldnt(kp + r * TSTR + t * 512); } SB(); } \
      _Pragma("unroll") for (int t = 0; t < 4; ++t) s[t] = row_sum(dot2(KB[t][3], q[3], dot2(KB[t][2], q[2], dot2(KB[t][1], q[1], dot2(KB[t][0], q[0], 0.f))))); \
      { unsigned a = pk_bf16(s[0], s[1]), c = pk_bf16(s[2], s[3]); \
        a = pk_bf16(bf_lo(a) * SCALE, bf_hi(a) * SCALE); c = pk_bf16(bf_lo(c) * SCALE, bf_hi(c) * SCALE); \
        s[0] = bf_lo(a); s[1] = bf_hi(a); s[2] = bf_lo(c); s[3] = bf_hi(c); } \
      const float mn = fmaxf(fmaxf(fmaxf(s[0], s[1]), fmaxf(s[2], s[3])), m); \
      const float al = __builtin_amdgcn_exp2f((m - mn) * LOG2E); m = mn; \
      _Pragma("unroll") for (int t = 0; t < 4; ++t) s[t] = __builtin_amdgcn_exp2f((s[t] - mn) * LOG2E); \
      l = fmaf(l, al, (s[0] + s[1]) + (s[2] + s[3])); \
      _Pragma("unroll") for (int j = 0; j < 8; ++j) acc[j] *= al; \
      asm volatile("" : "+v"(s[0]), "+v"(s[1]), "+v"(s[2]), "+v"(s[3]), "+v"(l)); \
      if (ISSUE) { SB(); PACE(); _Pragma("unroll") for (int t = 0; t < 4; ++t) { VMCNT(THR - 1); vb[r][t] = ldnt(vp + r * TSTR + t * 512); } \
        if (r == 3 && it == 5 && lane == 0) lds_st(ctl + 14, 1u);       \
        SB(); } \
      _Pragma("unroll") for (int t = 0; t < 4; ++t) { \
        _Pragma("unroll") for (int j = 0; j < 4; ++j) { acc[2 * j] = fmaf(s[t], bf_lo(VB[t][j]), acc[2 * j]); acc[2 * j + 1] = fmaf(s[t], bf_hi(VB[t][j]), acc[2 * j + 1]); } } \
      asm volatile("" : "+v"(acc[0]), "+v"(acc[1]), "+v"(acc[2]), "+v"(acc[3]), "+v"(acc[4]), "+v"(acc[5]), "+v"(acc[6]), "+v"(acc[7])); \
      SB(); \
    } while (0)
    int it = 0;
    TILE(kb[0], vb[0], 0, false); TILE(kb[1], vb[1], 0, false);
#pragma unroll 1
    for (; it < 6; ++it) {
      TILE(kb[2], vb[2], 0, true); TILE(kb[3], vb[3], 1, true); TILE(kb[0], vb[0], 2, true); TILE(kb[1], vb[1], 3, true);
      kp += 4 * TSTR; vp += 4 * TSTR;
      if (it < 3 + nx) {
        u32x4 kl[4], vl[4];
        LAS unsigned char* src = lds + L_KV + wave * 32768 + (it & 3) * 8192 + lane * 16;
#pragma unroll
        for (int t = 0; t < 4; ++t) { kl[t] = *(LAS u32x4*)(src + t * 1024); vl[t] = *(LAS u32x4*)(src + 4096 + t * 1024); }
        TILE(kl, vl, 0, false);
        if (it == 2 + nx && lane == 0) __hip_atomic_fetch_add(ctl + 13, 1u, RLX_WG);
        if (it == 1 && nx == 2) PARK(0, xo - 15 * TSTR + 64 * DH);
      }
    }
    TILE(kb[2], vb[2], 0, false); TILE(kb[3], vb[3], 0, false);
#undef TILE
    const float M = xrow_max(m), f = __builtin_amdgcn_exp2f((m - M) * LOG2E);
    l = xrow_sum(l * f);
#pragma unroll
    for (int j = 0; j < 8; ++j) acc[j] = xrow_sum(acc[j] * f);
    LAS float* rec = (LAS float*)(lds + L_MRG) + wave * 132;
    if (lane < 16) {
      *(LAS f32x4*)(rec + 4 + lane * 8) = f32x4{acc[0], acc[1], acc[2], acc[3]};
      *(LAS f32x4*)(rec + 8 + lane * 8) = f32x4{acc[4], acc[5], acc[6], acc[7]};
      if (lane == 0) *(LAS f32x2*)rec = f32x2{M, l};
    }
    WAIT_LGKM0();
    unsigned old = 0;
    if (lane == 0) old = __hip_atomic_fetch_add(ctl + 1, 1u, RLX_WG);
    old = __builtin_amdgcn_readfirstlane(old);
    if (old != 3) return;
    asm volatile("" ::: "memory");
    LAS float* mr = (LAS float*)(lds + L_MRG);
    float mw[4], lw[4]; f32x4 av[4];
#pragma unroll
    for (int w = 0; w < 4; ++w) { f32x2 t = *(LAS f32x2*)(mr + w * 132); mw[w] = t[0]; lw[w] = t[1]; av[w] = *(LAS f32x4*)(mr + w * 132 + 4 + 4 * (lane & 31)); }
    const unsigned ta = ctl[4], tb = ctl[5];
    const float MM = fmaxf(fmaxf(mw[0], mw[1]), fmaxf(mw[2], mw[3]));
    float L = 0.f; f32x4 o = {0.f, 0.f, 0.f, 0.f};
#pragma unroll
    for (int w = 0; w < 4; ++w) { const float g = __builtin_amdgcn_exp2f((mw[w] - MM) * LOG2E); L = fmaf(g, lw[w], L); o += g * av[w]; }
    if (lane == 32) o = f32x4{MM, 0.f, L, 0.f};
    if (lane <= 32) GST(o, WS_OX + gid * 640 + lane * 16);
    return;
  }

  const int aw = wave - 4, g4 = lane >> 4, c16 = lane & 15;
  const unsigned ts = NOW16();
  u64 E = 0;
  unsigned ta = 0, tb = 0;
  unsigned msk[4];
#pragma unroll
  for (int d = 0; d < 4; ++d) msk[d] = ((c16 & 7) >> 1) == d ? ((c16 & 1) ? 0xffff0000u : 0x0000ffffu) : 0u;
  const unsigned sel = (c16 & 8) ? 0x03020302u : 0x01000100u;
  LAS unsigned char* xt = lds + L_XT + aw * 448;
#define PROJECT(NL, W, REDOFF, CNT, DSTOFF, WIDE) do { \
    f32x4 d0 = {0.f, 0.f, 0.f, 0.f}, d1 = {0.f, 0.f, 0.f, 0.f}; \
    _Pragma("unroll") for (int i = 0; i < NL; ++i) { \
      const u32x2 xx = *(LAS u32x2*)(xt + (i * 4 + g4) * 8); \
      const unsigned x0 = __builtin_amdgcn_perm(xx[0], xx[0], sel), x1 = __builtin_amdgcn_perm(xx[1], xx[1], sel); \
      d0 = mfma(__builtin_bit_cast(bf16x8, W[i]), __builtin_bit_cast(bf16x8, u32x4{x0 & msk[0], x0 & msk[1], x0 & msk[2], x0 & msk[3]}), d0); \
      d1 = mfma(__builtin_bit_cast(bf16x8, W[i]), __builtin_bit_cast(bf16x8, u32x4{x1 & msk[0], x1 & msk[1], x1 & msk[2], x1 & msk[3]}), d1); \
    } \
    LAS f32x4* red = (LAS f32x4*)(lds + L_RED + REDOFF); \
    red[(aw * 2 + 0) * 64 + lane] = d0; red[(aw * 2 + 1) * 64 + lane] = d1; \
    WAIT_LGKM0(); \
    unsigned old = 0; \
    if (lane == 0) old = __hip_atomic_fetch_add(ctl + CNT, 1u, RLX_WG); \
    old = __builtin_amdgcn_readfirstlane(old); \
    lds_wait_ge<0>(ctl + 6, 1); ta = ctl[4]; tb = ctl[5]; \
    if (old == 3) {     \
      d0 = (red[lane] + red[128 + lane]) + (red[256 + lane] + red[384 + lane]); \
      d1 = (red[64 + lane] + red[192 + lane]) + (red[320 + lane] + red[448 + lane]); \
      if (WIDE) { \
        const int off = (DSTOFF) + (c16 >> 3) * 1024 + ((c16 & 7) * 4 + g4) * 16; \
        GSTW(d0[0], d0[1], off); GSTW(d0[2], d0[3], off + 512); GSTW(d1[0], d1[1], off + 2048); GSTW(d1[2], d1[3], off + 2560); \
      } else { \
        const int off = (DSTOFF) + (c16 >> 3) * 512 + ((c16 & 7) * 4 + g4) * 16; \
        GST(d0, off); GST(d1, off + 1024); \
      } \
    } } while (0)
#define XTABLE(NROWS2) do { \
    u32x4 t = {__builtin_amdgcn_perm(xr[1], xr[0], 0x05040100u), __builtin_amdgcn_perm(xr[3], xr[2], 0x05040100u), \
               __builtin_amdgcn_perm(xr[1], xr[0], 0x07060302u), __builtin_amdgcn_perm(xr[3], xr[2], 0x07060302u)}; \
    if (lane >= (NROWS2)) t = u32x4{0u, 0u, 0u, 0u}; \
    if (lane < 28) *(LAS u32x4*)(xt + lane * 16) = t; \
    WAIT_LGKM0(); } while (0)
  {
    const int r0 = mem * 80 + aw * 20;
    u32x4 w[5]; unsigned xr[4];
#pragma unroll
    for (int i = 0; i < 5; ++i) w[i] = ldnt(p.wq + (size_t)(r0 + i * 4 + g4) * (NH * DH) + h * DH + c16 * 8);
#pragma unroll
    for (int bb = 0; bb < 4; ++bb) xr[bb] = ldnt32(p.x + bb * DM + r0 + 2 * min(lane, 9));
    if (aw == 3) E = __hip_atomic_load((gu64*)(p.ws + WS_E), RLX_AGENT);
    if (aw == 0 && lane < 16) lds_st(ctl + lane, 0u);
    WAIT_LGKM0();
    __builtin_amdgcn_s_barrier();
    XTABLE(10);
    asm volatile("" : "+v"(E));
    E += 1;
    if (aw == 3 && lane == 0) { ctl[4] = (unsigned)E << 12; ctl[5] = ((unsigned)(E >> 20) + (unsigned)E * 0x9E5u) << 12; WAIT_LGKM0(); lds_st(ctl + 6, 1u); }
    const unsigned odd = __builtin_amdgcn_s_getreg(20 | (3 << 11)) & 1u;
    if (aw == 3) { ta = ctl[4]; tb = ctl[5]; if (lane == 0) GST((f32x4{odd ? 1.f : 0.f, 0.f, 0.f, 0.f}), WS_OX + gid * 640 + 33 * 16); }
    PROJECT(5, w, 0, 7, WS_QX + gid * 4096, true);
    if (aw == 2) {
      u32x4 v;
      _Pragma("nounroll") for (unsigned n = 0; n < SPIN_CAP; ++n) {
        asm volatile("" ::: "memory");
        v = GLD(WS_OX + (gid ^ 1) * 640 + 33 * 16);
        if (__all(gr_ok(v, ta, tb))) break;
        __builtin_amdgcn_s_sleep(8);
      }
      const unsigned po = (v[0] << 10) != 0u;
      if (lane == 0) lds_st(ctl + 15, po == odd ? 2u : odd ? 1u : 3u);
    }
    if (aw == 3) {
      const int off = WS_QX + h * 32768 + b * 1024 + lane * 16;
      const unsigned wa = ta | (tb >> 20), wb = tb | (ta >> 20);
      u32x4 v[8];
      __builtin_amdgcn_s_sleep(QDELAY);
      _Pragma("nounroll") for (unsigned n = 0; n < SPIN_CAP; ++n) {
        bool ok = true;
        asm volatile("" ::: "memory");
#pragma unroll
        for (int i = 0; i < 8; ++i) v[i] = GLD(off + i * 4096);
#pragma unroll
        for (int i = 0; i < 8; ++i) ok &= v[i][1] == wa && v[i][3] == wb;
        if (__all(ok)) break;
      }
      float q0 = 0.f, q1 = 0.f;
#pragma unroll
      for (int i = 0; i < 8; ++i) { q0 += __uint_as_float(v[i][0]); q1 += __uint_as_float(v[i][2]); }
      const unsigned qq = pk_bf16(q0, q1);
      LAS unsigned char* qd = lds + L_Q + ((lane & 3) * 32 + (lane >> 5) * 16 + ((lane >> 2) & 7)) * 2;
      *(LAS u16*)qd = (u16)qq; *(LAS u16*)(qd + 16) = (u16)(qq >> 16);
      WAIT_LGKM0();
      if (lane == 0) lds_st(ctl + 0, 1u);
    }
    lds_wait_ge<4>(ctl + 0, 1);
  }
  if ((mem & 1) && mem > 1) {
    if (h >= 27) return;
    const int part = (mem - 3) >> 1, nl = part == 0 ? 14 : 13;
    const int r0 = (part == 0 ? 0 : 16 + part * 208) + aw * nl * 4;
    u32x4 w[14]; unsigned xr[4];
#pragma unroll
    for (int bb = 0; bb < 4; ++bb) xr[bb] = ldnt32(p.x + bb * DM + r0 + 2 * min(lane, nl * 2 - 1));
    const u16* src = (c16 < 8 ? p.wg : p.wu) + h * 64 + (c16 & 7) * 8;
#pragma unroll
    for (int i = 0; i < 14; ++i) { __builtin_amdgcn_s_sleep(APACE); VMCNT(3); w[i] = ldnt(src + (size_t)(r0 + min(i, nl - 1) * 4 + g4) * FF); }
    XTABLE(nl * 2);
    PROJECT(14, w, 8192, 12, WS_GX + (h * 3 + part) * 2048, false);
    return;
  }

  const int cc = mem < 2 ? mem : (mem >> 1) + 1;
  const unsigned trq = (lane & 15) >> 2, trp = lane & 3;
  if (aw == 1) {
    unsigned gwv[5], xg[4][5];
#pragma unroll
    for (int i = 0; i < 5; ++i) {
      gwv[i] = ldnt32(p.gw + i * 128 + lane * 2);
#pragma unroll
      for (int bb = 0; bb < 4; ++bb) xg[bb][i] = ldnt32(p.x + bb * DM + i * 128 + lane * 2);
    }
#pragma unroll
    for (int bb = 0; bb < 4; ++bb) {
      float s = 0.f;
#pragma unroll
      for (int i = 0; i < 5; ++i) { const unsigned pr = pk_bf16(bf_lo(xg[bb][i]) * bf_lo(gwv[i]), bf_hi(xg[bb][i]) * bf_hi(gwv[i])); s += bf_lo(pr) + bf_hi(pr); }
      s = rbf(xrow_sum(row_sum(s)));
      const float gt = rbf(1.f / (1.f + expf(-s)));
      if (lane == 0) *(LAS float*)(lds + L_CTL + 32 + bb * 4) = gt;
    }
  } else if (aw == 3 && h < 27) {
    const int off = WS_GX + h * 6144 + lane * 16;
    u32x4 v[6];
    _Pragma("nounroll") while ((int)(NOW16() - ts - (unsigned)(GWAIT * 1600)) < 0) __builtin_amdgcn_s_sleep(64);
    _Pragma("nounroll") for (unsigned n = 0; n < SPIN_CAP; ++n) {
      bool ok = true;
      if (n) __builtin_amdgcn_s_sleep(32);
      asm volatile("" ::: "memory");
#pragma unroll
      for (int i = 0; i < 6; ++i) v[i] = GLD(off + i * 1024);
#pragma unroll
      for (int i = 0; i < 6; ++i) ok &= gr_ok(v[i], ta, tb);
      if (__all(ok)) break;
    }
#pragma unroll
    for (int u = 0; u < 2; ++u) {
      const f32x4 s = (gr_val(v[u]) + gr_val(v[2 + u])) + gr_val(v[4 + u]);
#pragma unroll
      for (int r = 0; r < 4; ++r) {
        const float a = rbf(s[r]), hv = a * dpp<0x4E>(a);
        if ((lane & 2) == 0) *(LAS u16*)(lds + L_H + (u * 2 + (lane >> 5)) * 144 + (((lane & 1) * 4 + r) * 8 + ((lane >> 2) & 7)) * 2) = (u16)pk_bf16(hv, 0.f);
      }
    }
  }
  {
    lds_wait_ge<4>(ctl + 14, 1);
    u32x4 wv[8], wd[4];
#pragma unroll
    for (int i = 0; i < 8; ++i) wv[i] = ldnt(p.wo + (size_t)(h * DH + aw * 32 + i * 4 + g4) * DM + cc * 128 + c16 * 8);
    if (h < 27) {
#pragma unroll
      for (int i = 0; i < 4; ++i) wd[i] = ldnt(p.wd + (size_t)(h * 64 + aw * 16 + i * 4 + g4) * DM + cc * 128 + c16 * 8);
    }
    lds_wait_ge<1>(ctl + 13, 4);
#pragma unroll
    for (int i = 0; i < 8; ++i) *(LAS u32x4*)(lds + L_WO + (aw * 32 + i * 4 + g4) * 272 + c16 * 16) = wv[i];
    if (h < 27) {
#pragma unroll
      for (int i = 0; i < 4; ++i) *(LAS u32x4*)(lds + L_WD + (aw * 16 + i * 4 + g4) * 272 + c16 * 16) = wd[i];
    }
  }
  WAIT_LGKM0();
  if (lane == 0) __hip_atomic_fetch_add(ctl + 2, 1u, RLX_WG);
  lds_wait_ge<0>(ctl + 2, 4);
  bf16x8 bo[2][4];
  f32x4 accz[2] = {f32x4{0.f, 0.f, 0.f, 0.f}, f32x4{0.f, 0.f, 0.f, 0.f}};
  {
    LAS unsigned char* tw = lds + L_WO + (g4 * 8 + trq) * 272 + trp * 8 + aw * 64;
#pragma unroll
    for (int ks = 0; ks < 4; ++ks) { bo[0][ks] = tr_frag(tw + ks * 8704, tw + ks * 8704 + 1088); bo[1][ks] = tr_frag(tw + ks * 8704 + 32, tw + ks * 8704 + 1088 + 32); }
    if (h < 27) {
      LAS unsigned char* td = lds + L_WD + (g4 * 8 + trq) * 272 + trp * 8 + aw * 64;
#pragma unroll
      for (int ks = 0; ks < 2; ++ks) {
        const bf16x8 af = *(LAS bf16x8*)(lds + L_H + (lane & 3) * 144 + ks * 64 + g4 * 16);
        accz[0] = mfma(af, tr_frag(td + ks * 8704, td + ks * 8704 + 1088), accz[0]);
        accz[1] = mfma(af, tr_frag(td + ks * 8704 + 32, td + ks * 8704 + 1088 + 32), accz[1]);
      }
    }
  }
  f32x4 gate = *(LAS f32x4*)(lds + L_CTL + 32);
#pragma unroll
  for (int i = 0; i < 4; ++i) { const float og = rbf(1.f - gate[i]); accz[0][i] *= og; accz[1][i] *= og; }
  asm volatile("" : "+v"(accz[0]), "+v"(accz[1]), "+v"(gate));
  {
    const int off = WS_OX + (h * 8 + aw * 2) * 640;
    u32x4 v0, v1, v2;
    _Pragma("nounroll") for (unsigned n = 0; n < SPIN_CAP * 4; ++n) {
      asm volatile("" ::: "memory");
      v0 = GLD(off + (lane >> 5) * 640 + (lane & 31) * 16); v1 = GLD(off + 512); v2 = GLD(off + 640 + 512);
      if (__all(gr_ok(v0, ta, tb) && gr_ok(v1, ta, tb) && gr_ok(v2, ta, tb))) break;
    }
    const float m0 = __uint_as_float(v1[0] << 10), m1 = __uint_as_float(v2[0] << 10);
    const float MM = fmaxf(m0, m1), e0 = __builtin_amdgcn_exp2f((m0 - MM) * LOG2E), e1 = __builtin_amdgcn_exp2f((m1 - MM) * LOG2E);
    const float inv = __builtin_amdgcn_rcpf(fmaf(e0, __uint_as_float(v1[2] << 10), e1 * __uint_as_float(v2[2] << 10)));
    f32x4 o = gr_val(v0) * ((lane < 32 ? e0 : e1) * inv);
#pragma unroll
    for (int r = 0; r < 4; ++r) o[r] = half_sum(o[r]);
    if (lane < 32) *(LAS u32x2*)(lds + L_O + aw * 272 + lane * 8) = u32x2{pk_bf16(o[0], o[1]), pk_bf16(o[2], o[3])};
    WAIT_LGKM0();
    if (lane == 0) __hip_atomic_fetch_add(ctl + 2, 1u, RLX_WG);
  }
  lds_wait_ge<0>(ctl + 2, 8);
  {
    f32x4 d0 = {0.f, 0.f, 0.f, 0.f}, d1 = {0.f, 0.f, 0.f, 0.f};
#pragma unroll
    for (int ks = 0; ks < 4; ++ks) {
      const bf16x8 af = *(LAS bf16x8*)(lds + L_O + (lane & 3) * 272 + ks * 64 + g4 * 16);
      d0 = mfma(af, bo[0][ks], d0); d1 = mfma(af, bo[1][ks], d1);
    }
    const int off = WS_YX + (h * 5 + cc) * 2048 + aw * 512 + lane * 16;
    if (lane < 16) { GST(gate * d0 + accz[0], off); GST(gate * d1 + accz[1], off + 256); }
  }
  if (h >= 8) return;
  {
    for (int i = 3 - aw; i > 0; --i) __builtin_amdgcn_s_sleep(RSTAG);
    const int off = WS_YX + (g4 * 5 + cc) * 2048 + h * 256 + c16 * 16;
    u32x4 v[8];
    _Pragma("nounroll") for (unsigned n = 0; n < SPIN_CAP * 4; ++n) {
      bool ok = true;
      asm volatile("" ::: "memory");
#pragma unroll
      for (int i = 0; i < 8; ++i) v[i] = GLD(off + i * 20 * 2048);
#pragma unroll
      for (int i = 0; i < 8; ++i) ok &= gr_ok(v[i], ta, tb);
      if (__all(ok)) break;
    }
    f32x4 s = gr_val(v[0]);
#pragma unroll
    for (int i = 1; i < 8; ++i) s += gr_val(v[i]);
    const float s0 = xrow_sum(s[0]), s1 = xrow_sum(s[1]), s2 = xrow_sum(s[2]), s3 = xrow_sum(s[3]);
    const float r = g4 == 0 ? s0 : g4 == 1 ? s1 : g4 == 2 ? s2 : s3;
    p.out[g4 * DM + cc * 128 + h * 16 + c16] = (u16)(pk_bf16(r, 0.f) & 0xffffu);
    if (gid == 0 && aw == 3 && lane == 0) __hip_atomic_store((gu64*)(p.ws + WS_E), E, RLX_AGENT);
  }
}

extern "C" void kernel_launch(void* const* d_in, const int* in_sizes, int n_in, void* d_out, int out_size, void* d_ws, size_t ws_size,
                              hipStream_t stream) {
  Params p;
  p.x = (const u16*)d_in[0]; p.k = (const u16*)d_in[1]; p.v = (const u16*)d_in[2]; p.wq = (const u16*)d_in[3]; p.wo = (const u16*)d_in[4];
  p.wg = (const u16*)d_in[6]; p.wu = (const u16*)d_in[7]; p.wd = (const u16*)d_in[8]; p.gw = (const u16*)d_in[9];
  p.out = (u16*)d_out; p.ws = (unsigned char*)d_ws;
  hipLaunchKernelGGL(fused_kernel, dim3(NWG), dim3(512), 0, stream, p);
}
```
